```python
import torch

def _softmax(x, axis=-1):
    return torch.softmax(x, dim=axis)
def _gelu(x):
    return torch.nn.functional.gelu(x, approximate="tanh")
def _silu(x):
    return torch.nn.functional.silu(x)
def _sigmoid(x):
    return torch.sigmoid(x)
def _layernorm(x, axis=-1, eps=1e-5):
    m = x.mean(dim=axis, keepdim=True)
    v = x.var(dim=axis, keepdim=True, unbiased=False)
    return (x - m) / torch.sqrt(v + eps)
def _scatter_add(tgt, idx, src, axis=0):
    return tgt.clone().index_add_(axis, idx, src)
def _segment_sum(src, seg, n):
    out = torch.zeros((n,) + src.shape[1:], dtype=src.dtype)
    return out.index_add_(0, seg, src)
def _pad(x, pw):
    flat = tuple(v for pair in reversed(pw) for v in pair)
    return torch.nn.functional.pad(x, flat)

kernel_name = 'recurrent_scan__spectral_gfnet__dense_activation_squared_relu__prompt'

def setup_inputs():

    _w = (lambda a: (lambda t: t.to(torch.bfloat16) if t.is_floating_point() else t)(torch.as_tensor(a)))
    def _A_setup():
        B, S, D = 1, 2048, 576
        Df = D // 2 + 1
        return {
            "x": _w((lambda *s: torch.randn(*s, dtype=torch.bfloat16) * 0.05)(B, S, D)),
            "w_r": _w((lambda *s: torch.randn(*s, dtype=torch.bfloat16) * 0.05)(S, Df) * 0.02),
            "w_i": _w((lambda *s: torch.randn(*s, dtype=torch.bfloat16) * 0.05)(S, Df) * 0.02),
        }
    _A_in = _A_setup()

    def _B_setup():
        B, S, D, F = 1, 2048, 576, 4608
        return {
            "x": _w((lambda *s: torch.randn(*s, dtype=torch.bfloat16) * 0.05)(B, S, D)),
            "w1": _w((lambda *s: torch.randn(*s, dtype=torch.bfloat16) * 0.05)(D, F)),
            "b1": _w((lambda *s: torch.randn(*s, dtype=torch.bfloat16) * 0.4)(F)),
            "w2": _w((lambda *s: torch.randn(*s, dtype=torch.bfloat16) * 0.05)(F, D)),
            "b2": _w((lambda *s: torch.randn(*s, dtype=torch.bfloat16) * 3.2)(D)),
        }
    _B_in = _B_setup()

    return {
        "A_x": _A_in["x"],
        "A_w_r": _A_in["w_r"],
        "A_w_i": _A_in["w_i"],
        "B_x": _B_in["x"],
        "B_w1": _B_in["w1"],
        "B_b1": _B_in["b1"],
        "B_w2": _B_in["w2"],
        "B_b2": _B_in["b2"],
    }

def reference(A_x, A_w_r, A_w_i, B_x, B_w1, B_b1, B_w2, B_b2):
    def _A_ref(x, w_r, w_i):
        B, S, D = x.shape
        X = (lambda x, axes=(-2,-1): torch.fft.rfft2(x.float(), dim=axes))(x, axes=(-2, -1))
        W = w_r + 1j * w_i
        X = X * W
        y = (lambda x, s, axes=(-2,-1): torch.fft.irfft2(x, s=s, dim=axes).to(torch.bfloat16))(X, (S, D), axes=(-2, -1))
        return y

    def _B_ref(x, w1, b1, w2, b2):
        h = torch.einsum("bsd,df->bsf", x, w1) + b1
        zero = torch.zeros_like(h)
        r = torch.where(h > zero, h, zero)
        h = r * r
        out = torch.einsum("bsf,fd->bsd", h, w2) + b2
        return out

    x = A_x
    xn = _layernorm(x, axis=-1)
    s = _A_ref(xn, A_w_r, A_w_i)
    g = _silu(_B_ref(xn, B_w1, B_b1, B_w2, B_b2))
    return x + s * g
```

```cpp
#include <hip/hip_runtime.h>
#include <hip/hip_bf16.h>
#include <stdint.h>

#if defined(__HIP_DEVICE_COMPILE__)
#define NOPK __attribute__((target("no-packed-fp32-ops")))
#else
#define NOPK
#endif
#define DI __device__ __forceinline__ NOPK
#define LAS __attribute__((address_space(3)))
typedef unsigned u32;
typedef unsigned short u16;
typedef u32 u32x2 __attribute__((ext_vector_type(2)));
typedef u32 u32x4 __attribute__((ext_vector_type(4)));
typedef float f32x2 __attribute__((ext_vector_type(2)));
typedef float f32x4 __attribute__((ext_vector_type(4)));
typedef float f32x16 __attribute__((ext_vector_type(16)));
typedef short s16x4 __attribute__((ext_vector_type(4)));
typedef __bf16 bf16x2 __attribute__((ext_vector_type(2)));
typedef __bf16 bf16x8 __attribute__((ext_vector_type(8)));
typedef _Float16 f16x2 __attribute__((ext_vector_type(2)));
typedef _Float16 f16x8 __attribute__((ext_vector_type(8)));

constexpr int S_ = 2048, D_ = 576, F_ = 4608, NKF = 289;

namespace tab {
constexpr double PI = 3.14159265358979323846264338327950288;
constexpr double ccos(int n, int den) {
  if (2 * n > den) n = den - n;
  double sg = 1.0;
  if (4 * n > den) { n = den / 2 - n; sg = -1.0; }
  if (4 * n == den) return 0.0;
  const double x = 2.0 * PI * n / den, x2 = x * x;
  double term = 1.0, sum = 1.0;
  for (int i = 1; i <= 12; ++i) { term *= -x2 / ((2 * i - 1) * (2 * i)); sum += term; }
  return sg * sum;
}
template <int N> struct Cos { float v[N]; };
template <int N> constexpr Cos<N> mkcos() { Cos<N> c{}; for (int i = 0; i < N; ++i) c.v[i] = (float)ccos(i, N); return c; }
constexpr Cos<576> C576 = mkcos<576>();
constexpr Cos<2048> C2048 = mkcos<2048>();
constexpr float c576(int n) { return C576.v[n % 576]; }
constexpr float s576(int n) { return C576.v[(n + 432) % 576]; }
constexpr float c2048(int n) { return C2048.v[n % 2048]; }
constexpr float s2048(int n) { return C2048.v[(n + 1536) % 2048]; }
constexpr u16 tobf(float f) { u32 u = __builtin_bit_cast(u32, f); u += 0x7fffu + ((u >> 16) & 1u); return (u16)(u >> 16); }
constexpr u16 tohf(float f) { return __builtin_bit_cast(u16, (_Float16)f); }
constexpr int crow(int reg, int h) { return (reg & 3) + 8 * (reg >> 2) + 4 * h; }

template <int N> struct alignas(16) Frags { u16 v[N][64][8]; };
template <int N> struct alignas(16) F4s { float v[N][64][4]; };

constexpr Frags<4> mk_rf1() {
  Frags<4> t{};
  for (int f = 0; f < 4; ++f) for (int l = 0; l < 64; ++l) for (int j = 0; j < 8; ++j) {
    const int d1 = 16 * (f & 1) + 8 * (l >> 5) + j, k1 = l & 31;
    float v = 0.f;
    if (d1 < 18 && k1 < 18) v = (f < 2) ? c576(32 * d1 * k1) : -s576(32 * d1 * k1);
    t.v[f][l][j] = tohf(v);
  }
  return t;
}
constexpr F4s<8> mk_rft() {
  F4s<8> t{};
  for (int e = 0; e < 8; ++e) for (int l = 0; l < 64; ++l) for (int q = 0; q < 2; ++q) {
    const int d2 = crow(2 * e + q, l >> 5), k1 = l & 31;
    t.v[e][l][2 * q] = c576(d2 * k1); t.v[e][l][2 * q + 1] = s576(d2 * k1);
  }
  return t;
}
constexpr Frags<4> mk_rf2() {
  Frags<4> t{};
  for (int s = 0; s < 4; ++s) for (int l = 0; l < 64; ++l) for (int j = 0; j < 8; ++j) {
    const int d2 = crow(4 * s + (j >> 1), l >> 5), c = j & 1, k2 = (l & 31) >> 1, co = l & 1;
    const float cs = c576(18 * d2 * k2), sn = s576(18 * d2 * k2);
    t.v[s][l][j] = tohf(co == 0 ? (c == 0 ? cs : sn) : (c == 0 ? -sn : cs));
  }
  return t;
}
constexpr Frags<4> mk_mid() {
  Frags<4> t{};
  for (int l = 0; l < 64; ++l) for (int j = 0; j < 8; ++j) {
    const int idx = 4 * (l >> 4) + (j >> 1), c = j & 1, o = l & 15;
    const float cs = c2048(128 * idx * o), sn = s2048(128 * idx * o);
    t.v[0][l][j] = tohf(c == 0 ? cs : sn);
    t.v[1][l][j] = tohf(c == 0 ? -sn : cs);
    t.v[2][l][j] = tobf(c == 0 ? cs : -sn);
    t.v[3][l][j] = tobf(c == 0 ? sn : cs);
  }
  return t;
}
constexpr F4s<2> mk_midt() {
  F4s<2> t{};
  for (int e = 0; e < 2; ++e) for (int l = 0; l < 64; ++l) for (int q = 0; q < 2; ++q) {
    const int r = 4 * (l >> 4) + 2 * e + q, c = l & 15;
    t.v[e][l][2 * q] = c2048(8 * r * c); t.v[e][l][2 * q + 1] = s2048(8 * r * c);
  }
  return t;
}
constexpr Frags<4> mk_ri1() {
  Frags<4> t{};
  for (int f = 0; f < 4; ++f) for (int l = 0; l < 64; ++l) for (int j = 0; j < 8; ++j) {
    const int k2 = 8 * (f & 1) + 4 * (l >> 5) + (j >> 1), c = j & 1, d2 = l & 31;
    const float cs = c576(18 * d2 * k2), sn = s576(18 * d2 * k2);
    t.v[f][l][j] = tobf(f < 2 ? (c == 0 ? cs : -sn) : (c == 0 ? sn : cs));
  }
  return t;
}
constexpr F4s<5> mk_rit() {
  F4s<5> t{};
  for (int e = 0; e < 5; ++e) for (int l = 0; l < 64; ++l) for (int q = 0; q < 2; ++q) {
    const int k1 = crow(2 * e + q, l >> 5), d2 = l & 31;
    t.v[e][l][2 * q] = c576(d2 * k1); t.v[e][l][2 * q + 1] = s576(d2 * k1);
  }
  return t;
}
constexpr Frags<3> mk_ri2() {
  Frags<3> t{};
  for (int s = 0; s < 3; ++s) for (int l = 0; l < 64; ++l) for (int j = 0; j < 8; ++j) {
    const int reg = 4 * s + (j >> 1), k1 = crow(reg, l >> 5), c = j & 1, d1 = l & 31;
    float v = 0.f;
    if (reg < 10 && k1 < 18 && d1 < 18) v = (c == 0) ? c576(32 * d1 * k1) : -s576(32 * d1 * k1);
    t.v[s][l][j] = tobf(v);
  }
  return t;
}
struct alignas(64) Tw8 { float v[256][16]; };
constexpr Tw8 mk_tw8() {
  Tw8 t{};
  for (int b = 0; b < 256; ++b) for (int m = 0; m < 8; ++m) { t.v[b][2 * m] = c2048(b * m); t.v[b][2 * m + 1] = s2048(b * m); }
  return t;
}
}
static __device__ const tab::Frags<4> T_RF1 = tab::mk_rf1();
static __device__ const tab::F4s<8> T_RFT = tab::mk_rft();
static __device__ const tab::Frags<4> T_RF2 = tab::mk_rf2();
static __device__ const tab::Frags<4> T_MID = tab::mk_mid();
static __device__ const tab::F4s<2> T_MIDT = tab::mk_midt();
static __device__ const tab::Frags<4> T_RI1 = tab::mk_ri1();
static __device__ const tab::F4s<5> T_RIT = tab::mk_rit();
static __device__ const tab::Frags<3> T_RI2 = tab::mk_ri2();
static __device__ const tab::Tw8 T_TW8 = tab::mk_tw8();

constexpr size_t Z_PITCH = 292 * 4;
constexpr size_t Z_OFF = 0, Z_BYTES = 256 * 8 * Z_PITCH;
constexpr size_t V_OFF = (Z_OFF + Z_BYTES + 4095) / 4096 * 4096, V_BYTES = (size_t)8 * 145 * 2048;
constexpr size_t P_OFF = (V_OFF + V_BYTES + 4095) / 4096 * 4096, P_BLK = 9216, P_BYTES = (size_t)32 * 64 * P_BLK;

#define SB() __builtin_amdgcn_sched_barrier(0)
#define WAIT_V(n) asm volatile("s_waitcnt vmcnt(%0)" ::"n"(n) : "memory")
#define WAIT_L0() asm volatile("s_waitcnt lgkmcnt(0)" ::: "memory")
DI void bar() { asm volatile("" ::: "memory"); __builtin_amdgcn_s_barrier(); asm volatile("" ::: "memory"); }
DI void bar_lds() { WAIT_L0(); bar(); }
template <int CTRL> DI float dppf(float v) { return __builtin_bit_cast(float, __builtin_amdgcn_update_dpp(0, __builtin_bit_cast(int, v), CTRL, 0xf, 0xf, true)); }
DI float sum8(float v) { v += dppf<0xB1>(v); v += dppf<0x4E>(v); v += dppf<0x141>(v); return v; }
DI float rdlane(float v, int l) { return __builtin_bit_cast(float, __builtin_amdgcn_readlane(__builtin_bit_cast(int, v), l)); }
DI u32 umin(u32 a, u32 b) { return a < b ? a : b; }
#define TID ((int)__builtin_amdgcn_workitem_id_x())
#define BID ((int)__builtin_amdgcn_workgroup_id_x())
DI float bflo(u32 v) { return __builtin_bit_cast(float, v << 16); }
DI float bfhi(u32 v) { return __builtin_bit_cast(float, v & 0xffff0000u); }
DI u32 pkbf(float a, float b) { bf16x2 v = {(__bf16)a, (__bf16)b}; return __builtin_bit_cast(u32, v); }
DI u32 pkhf(float a, float b) { f16x2 v = {(_Float16)a, (_Float16)b}; return __builtin_bit_cast(u32, v); }
DI float rbf(float a) { return bflo(pkbf(a, a)); }
DI float dot2(u32 a, u32 b, float c) { return __builtin_amdgcn_fdot2_f32_bf16(__builtin_bit_cast(bf16x2, a), __builtin_bit_cast(bf16x2, b), c, false); }
DI f32x4 mfma16b(u32x4 a, u32x4 b, f32x4 c) { return __builtin_amdgcn_mfma_f32_16x16x32_bf16(__builtin_bit_cast(bf16x8, a), __builtin_bit_cast(bf16x8, b), c, 0, 0, 0); }
DI f32x4 mfma16h(u32x4 a, u32x4 b, f32x4 c) { return __builtin_amdgcn_mfma_f32_16x16x32_f16(__builtin_bit_cast(f16x8, a), __builtin_bit_cast(f16x8, b), c, 0, 0, 0); }
DI f32x16 mfma32b(u32x4 a, u32x4 b, f32x16 c) { return __builtin_amdgcn_mfma_f32_32x32x16_bf16(__builtin_bit_cast(bf16x8, a), __builtin_bit_cast(bf16x8, b), c, 0, 0, 0); }
DI f32x16 mfma32h(u32x4 a, u32x4 b, f32x16 c) { return __builtin_amdgcn_mfma_f32_32x32x16_f16(__builtin_bit_cast(f16x8, a), __builtin_bit_cast(f16x8, b), c, 0, 0, 0); }
DI u32x2 trread(u32 addr) { return __builtin_bit_cast(u32x2, __builtin_amdgcn_ds_read_tr16_b64_v4i16((LAS s16x4*)(size_t)addr)); }
DI u32x4 ldfrag(const void* tbl, int f, int lane) { return ((const u32x4*)tbl)[f * 64 + lane]; }
DI f32x4 ldf4(const void* tbl, int f, int lane) { return ((const f32x4*)tbl)[f * 64 + lane]; }
DI void glds(const char* sbase, u32 voff, u32 dst) {
  asm volatile("s_mov_b32 m0, %2\n\ts_nop 0\n\tglobal_load_lds_dwordx4 %0, %1" ::"v"(voff), "s"(sbase), "s"(dst) : "memory");
}
DI float fmul1(float a, float b) { return a * b; }
DI float ffma1(float a, float b, float c) { return __builtin_fmaf(a, b, c); }
DI float fnma1(float a, float b, float c) { return __builtin_fmaf(-a, b, c); }
DI f32x2 cmul(float x, float y, float c, float s) { return f32x2{fnma1(y, s, fmul1(x, c)), ffma1(x, s, fmul1(y, c))}; }
DI f32x2 cmulc(float x, float y, float c, float s) { return f32x2{ffma1(y, s, fmul1(x, c)), fnma1(x, s, fmul1(y, c))}; }
DI u32 pkbf(f32x2 v) { return pkbf(v[0], v[1]); }
DI u32 pkhf(f32x2 v) { return pkhf(v[0], v[1]); }
DI void settle(f32x16& a, f32x16& b) { asm volatile("s_nop 7\n\ts_nop 7" : "+v"(a), "+v"(b)); }
DI void settle(f32x16& a) { asm volatile("s_nop 7\n\ts_nop 7" : "+v"(a)); }
DI void settle(f32x4& a, f32x4& b) { asm volatile("s_nop 7\n\ts_nop 1" : "+v"(a), "+v"(b)); }
template <typename T> DI T ldsr(u32 addr) { return *(const LAS T*)(size_t)addr; }
template <typename T> DI void ldsw(u32 addr, T v) { *(LAS T*)(size_t)addr = v; }

template <int SGN> DI void fft8(float (&r)[8], float (&i)[8]) {
  constexpr float R = 0.70710678118654752f;
  float ar[8], ai[8];
#pragma unroll
  for (int n = 0; n < 4; ++n) { ar[n] = r[n] + r[n + 4]; ai[n] = i[n] + i[n + 4]; ar[n + 4] = r[n] - r[n + 4]; ai[n + 4] = i[n] - i[n + 4]; }
  { float x = ar[5], y = ai[5]; ar[5] = (x - SGN * y) * R; ai[5] = (y + SGN * x) * R; }
  { float x = ar[6], y = ai[6]; ar[6] = -SGN * y; ai[6] = SGN * x; }
  { float x = ar[7], y = ai[7]; ar[7] = (-x - SGN * y) * R; ai[7] = (-y + SGN * x) * R; }
#pragma unroll
  for (int hlf = 0; hlf < 2; ++hlf) {
    const int o = 4 * hlf;
    const float b0r = ar[o] + ar[o + 2], b0i = ai[o] + ai[o + 2], b2r = ar[o] - ar[o + 2], b2i = ai[o] - ai[o + 2];
    const float b1r = ar[o + 1] + ar[o + 3], b1i = ai[o + 1] + ai[o + 3];
    const float tr = ar[o + 1] - ar[o + 3], ti = ai[o + 1] - ai[o + 3];
    const float b3r = -SGN * ti, b3i = SGN * tr;
    r[hlf] = b0r + b1r; i[hlf] = b0i + b1i;
    r[hlf + 4] = b0r - b1r; i[hlf + 4] = b0i - b1i;
    r[hlf + 2] = b2r + b3r; i[hlf + 2] = b2i + b3i;
    r[hlf + 6] = b2r - b3r; i[hlf + 6] = b2i - b3i;
  }
}

constexpr u32 L_RING = 0, SLOT = 24576, L_H = 3 * SLOT, L_AUX = L_H + 73728, L_ROWS = L_AUX, L_B1 = L_ROWS + 8 * 1152, L_END = L_B1 + 2304;
constexpr u32 ST_PITCH = 296 * 8;

struct WSrc { const char* base; u32 pitch; };

template <int PH>
DI void gemm_phase(const u32x4 (&st)[2][18], u32x2 (&wf)[3][2], u32 lds, int rot, int wid, int lane,
                   const char* wcur, const char* wnext, u32 voff_cur, u32 voff_next, u32 tr_cur, u32 tr_next,
                   char* pdst  ) {
  constexpr u32 PITCH = PH == 0 ? 9216u : 1152u, PITCHN = 1152u;
  constexpr u32 HI = PH == 0 ? 4 * 128u : 16 * 128u, HIN = 16 * 128u;
  const int ng = wid >> 1, mg = wid & 1;
  const u32 dstw = lds + L_RING + wid * 1024;
  for (int r = 0; r < 9; ++r) {
    int rc = r + rot; if (rc >= 9) rc -= 9;
    int rn = rc + 1; if (rn >= 9) rn -= 9;
    const bool last = (r == 8);
    const char* sc = wcur + rc * 128;
    const char* sn = last ? wnext + rn * 128 : wcur + rn * 128;
    const u32 pn = last ? PITCHN : PITCH;
    const u32 vn = last ? voff_next : voff_cur;
    const u32 trn = last ? tr_next : tr_cur;
    const u32 hin = last ? HIN : HI;
    f32x4 acc0, acc1, bias;
    if (PH == 0) bias = ldsr<f32x4>(lds + L_B1 + (rc * 64 + ng * 16 + (lane >> 4) * 4) * 4);
    else bias = f32x4{0.f, 0.f, 0.f, 0.f};
#pragma unroll
    for (int ks = 0; ks < 18; ++ks) {
      const int kr = ks + 2;
      if (kr % 6 == 0) {
        SB();
        WAIT_L0();
        if (PH == 1 && kr == 6) WAIT_V(4);
        else if (PH == 1 && kr == 12) { if (last) WAIT_V(1); else WAIT_V(4); }
        else WAIT_V(3);
        bar();
        SB();
        const int gi = kr / 6 + 2;
        if (!(PH == 1 && last)) {
#pragma unroll
          for (int u = 0; u < 3; ++u) glds(sn + (size_t)((gi - 3) * 192 + u * 64) * pn, vn, dstw + (gi % 3) * SLOT + u * 8192);
        } else { asm volatile("s_nop 0"); }
        SB();
      }
      if (kr < 18) {
        wf[kr % 3][0] = trread(tr_cur + (kr / 6) * SLOT + (kr % 6) * 4096);
        wf[kr % 3][1] = trread(tr_cur + (kr / 6) * SLOT + (kr % 6) * 4096 + HI);
      } else {
        wf[kr % 3][0] = trread(trn + (kr - 18) * 4096);
        wf[kr % 3][1] = trread(trn + hin + (kr - 18) * 4096);
      }
      SB();
      const u32x4 a = {wf[ks % 3][0][0], wf[ks % 3][0][1], wf[ks % 3][1][0], wf[ks % 3][1][1]};
      acc0 = mfma16b(a, st[0][ks], ks == 0 ? bias : acc0);
      acc1 = mfma16b(a, st[1][ks], ks == 0 ? bias : acc1);
      SB();
    }
    (void)sc;
    if (PH == 0) {
      const u32 ha = lds + L_H + ((2 * mg) * 18 + 2 * rc + (ng >> 1)) * 1024 + lane * 16 + (ng & 1) * 8;
      f32x4 p, q;
#pragma unroll
      for (int e = 0; e < 4; ++e) { p[e] = acc0[e] * __builtin_fmaxf(acc0[e], 0.f); q[e] = acc1[e] * __builtin_fmaxf(acc1[e], 0.f); }
      ldsw<u32x2>(ha, u32x2{pkbf(p[0], p[1]), pkbf(p[2], p[3])});
      ldsw<u32x2>(ha + 18 * 1024, u32x2{pkbf(q[0], q[1]), pkbf(q[2], q[3])});
    } else {
      const u32x4 v = {pkbf(acc0[0], acc0[1]), pkbf(acc0[2], acc0[3]), pkbf(acc1[0], acc1[1]), pkbf(acc1[2], acc1[3])};
      char* dst = pdst + rc * 1024;
      asm volatile("global_store_dwordx4 %0, %1, off\n\ts_nop 1" ::"v"(dst), "v"(v) : "memory");
    }
    SB();
  }
}

__global__ NOPK __launch_bounds__(512, 2) void k_main(const u16* __restrict__ x, const u16* __restrict__ w1, const u16* __restrict__ b1,
                                                 const u16* __restrict__ w2, char* __restrict__ ws) {
  extern __shared__ __attribute__((aligned(1024))) char smem[];
  const u32 lds = (u32)(size_t)smem;
  const int tid = TID, lane = tid & 63, wid = __builtin_amdgcn_readfirstlane(tid >> 6);
  const int j = BID & 7, i = BID >> 3, b = 32 * j + i;
  const int rot = i % 9;
  const char* w1c = (const char*)w1 + j * 1152;
  const char* w2c = (const char*)w2 + (size_t)j * 576 * 1152;
  const u32 lr = lane >> 3, lc = lane & 7;
  const u32 f1 = ((lr >> 1) & 1) | ((wid & 1) << 1), f2 = (lr >> 1) & 3;
  const u32 voff1 = (lr + wid * 8) * 9216u + ((((lc >> 1) ^ f1) << 1) | (lc & 1)) * 16;
  const u32 voff2 = (lr + wid * 8) * 1152u + ((((lc >> 1) ^ f2) << 1) | (lc & 1)) * 16;
  const u32 g = lane >> 4, q = (lane & 15) >> 2, p = lane & 3, ng = wid >> 1, mg = wid & 1;
  const u32 tr1 = lds + L_RING + (8 * g + q) * 128 + ((ng ^ ((q >> 1) | ((g & 1) << 1))) * 32) + p * 8;
  const u32 tr2 = lds + L_RING + (4 * g + q) * 128 + ((ng ^ ((2 * g + (q >> 1)) & 3)) * 32) + p * 8;
  {
    const u32 dstw = lds + L_RING + wid * 1024;
#pragma unroll
    for (int gi = 0; gi < 3; ++gi)
#pragma unroll
      for (int u = 0; u < 3; ++u) glds(w1c + rot * 128 + (size_t)(gi * 192 + u * 64) * 9216, voff1, dstw + gi * SLOT + u * 8192);
  }
  {
    const int a = lane >> 3;
    const u32x4* xr = (const u32x4*)(x + (size_t)(256 * a + 32 * wid + i) * D_) + lc;
    u32x4 v[9];
#pragma unroll
    for (int u = 0; u < 9; ++u) v[u] = xr[8 * u];
    if (tid < 288) { const u32 bb = ((const u32*)(b1 + j * 576))[tid]; ldsw<f32x2>(lds + L_B1 + tid * 8, f32x2{bflo(bb), bfhi(bb)}); }
    float s0 = 0.f, s1 = 0.f, q0 = 0.f, q1 = 0.f;
#pragma unroll
    for (int u = 0; u < 9; ++u) {
      s0 = dot2(v[u][0], 0x3f803f80u, s0); s1 = dot2(v[u][1], 0x3f803f80u, s1); s0 = dot2(v[u][2], 0x3f803f80u, s0); s1 = dot2(v[u][3], 0x3f803f80u, s1);
      q0 = dot2(v[u][0], v[u][0], q0); q1 = dot2(v[u][1], v[u][1], q1); q0 = dot2(v[u][2], v[u][2], q0); q1 = dot2(v[u][3], v[u][3], q1);
    }
    float sm = s0 + s1, sq = q0 + q1;
    sm = sum8(sm); sq = sum8(sq);
    const float mean = sm * (1.f / 576.f);
    const float var = __builtin_fmaxf(sq * (1.f / 576.f) - mean * mean, 0.f);
    const float mb = rbf(mean);
    const float inv = __builtin_amdgcn_rcpf(rbf(__builtin_amdgcn_sqrtf(rbf(rbf(var) + 1e-5f))));
    const int mt = 2 * (wid >> 2) + (a >> 2), r = 4 * (wid & 3) + (a & 3);
#pragma unroll
    for (int u = 0; u < 9; ++u) {
      u32x4 o;
#pragma unroll
      for (int e = 0; e < 4; ++e) {
        const u32 c = pkbf(bflo(v[u][e]) - mb, bfhi(v[u][e]) - mb);
        o[e] = pkbf(bflo(c) * inv, bfhi(c) * inv);
      }
      const int c = lc + 8 * u;
      ldsw<u32x4>(lds + L_H + (mt * 18 + (c >> 2)) * 1024 + ((c & 3) * 16 + r) * 16, o);
      if (wid == j) {
        u32x4 hh;
#pragma unroll
        for (int e = 0; e < 4; ++e) hh[e] = pkhf(bflo(o[e]), bfhi(o[e]));
        ldsw<u32x4>(lds + L_ROWS + a * 1152 + c * 16, hh);
      }
    }
  }
  bar_lds();
  f32x16 X;
  float nyq;
  {
    const u32 h = lane >> 5, blk = (lane >> 4) & 1;
    const u32 rb = lds + L_ROWS + wid * 1152 + blk * 32 + p * 8;
    const u32x2 a00 = trread(rb + (8 * h + q) * 64), a01 = trread(rb + (8 * h + 4 + q) * 64);
    const u32x2 a10 = trread(rb + umin(16 + 8 * h + q, 17u) * 64);
    const u32x4 A0 = {a00[0], a00[1], a01[0], a01[1]}, A1 = {a10[0], a10[1], a10[0], a10[1]};
    f32x16 z; for (int e = 0; e < 16; ++e) z[e] = 0.f;
    f32x16 tr = mfma32h(A0, ldfrag(&T_RF1, 0, lane), z); tr = mfma32h(A1, ldfrag(&T_RF1, 1, lane), tr);
    f32x16 ti = mfma32h(A0, ldfrag(&T_RF1, 2, lane), z); ti = mfma32h(A1, ldfrag(&T_RF1, 3, lane), ti);
    settle(tr, ti);
    float ny = 0.f;
#pragma unroll
    for (int e = 0; e < 16; e += 2) ny += tr[e] - tr[e + 1];
    nyq = rdlane(ny, 0) + rdlane(ny, 32);
    u32 pk[16];
#pragma unroll
    for (int e = 0; e < 8; ++e) {
      const f32x4 tw = ldf4(&T_RFT, e, lane);
      pk[2 * e] = pkhf(cmulc(tr[2 * e], ti[2 * e], tw[0], tw[1]));
      pk[2 * e + 1] = pkhf(cmulc(tr[2 * e + 1], ti[2 * e + 1], tw[2], tw[3]));
    }
    X = z;
#pragma unroll
    for (int s = 0; s < 4; ++s) X = mfma32h(ldfrag(&T_RF2, s, lane), u32x4{pk[4 * s], pk[4 * s + 1], pk[4 * s + 2], pk[4 * s + 3]}, X);
    settle(X);
  }
  SB();
  u32x4 st[2][18];
#pragma unroll
  for (int t = 0; t < 2; ++t)
#pragma unroll
    for (int ks = 0; ks < 18; ++ks) st[t][ks] = ldsr<u32x4>(lds + L_H + ((2 * mg + t) * 18 + ks) * 1024 + lane * 16);
  bar_lds();
  {
    const u32 k1 = lane & 31, h = lane >> 5;
    if (k1 < 18) {
#pragma unroll
      for (int e = 0; e < 8; ++e) {
        const u32 k2 = (e & 1) + 4 * (e >> 1) + 2 * h;
        ldsw<f32x2>(lds + L_H + wid * ST_PITCH + (k1 + 18 * k2) * 8, f32x2{X[2 * e], X[2 * e + 1]});
      }
    }
    if (lane == 0) ldsw<f32x2>(lds + L_H + wid * ST_PITCH + 288 * 8, f32x2{nyq, 0.f});
  }
  bar_lds();
  if (tid < NKF) {
    float re[8], im[8];
#pragma unroll
    for (int a = 0; a < 8; ++a) { const f32x2 v = ldsr<f32x2>(lds + L_H + a * ST_PITCH + tid * 8); re[a] = v[0]; im[a] = v[1]; }
    fft8<-1>(re, im);
    u32* zd = (u32*)(ws + Z_OFF + (size_t)b * 8 * Z_PITCH) + tid;
#pragma unroll
    for (int m = 0; m < 8; ++m) {
      const float c = T_TW8.v[b][2 * m] * (1.f / 64.f), s = T_TW8.v[b][2 * m + 1] * (1.f / 64.f);
      zd[m * 292] = pkhf(cmulc(re[m], im[m], c, s));
    }
  }
  WAIT_V(0);
  bar_lds();
  u32x2 wf[3][2];
#pragma unroll
  for (int kr = 0; kr < 2; ++kr) { wf[kr][0] = trread(tr1 + kr * 4096); wf[kr][1] = trread(tr1 + kr * 4096 + 512); }
  wf[2][0] = wf[0][0]; wf[2][1] = wf[0][1];
  char* pdst = ws + P_OFF + (size_t)((i * 8 + 4 * mg + q) * 8 + j) * P_BLK + ((ng * 4 + g) * 4 + p) * 16;
  gemm_phase<0>(st, wf, lds, rot, wid, lane, w1c, w2c, voff1, voff2, tr1, tr2, nullptr);
  bar_lds();
#pragma unroll
  for (int t = 0; t < 2; ++t)
#pragma unroll
    for (int ks = 0; ks < 18; ++ks) st[t][ks] = ldsr<u32x4>(lds + L_H + ((2 * mg + t) * 18 + ks) * 1024 + lane * 16);
  WAIT_V(0);
  gemm_phase<1>(st, wf, lds, rot, wid, lane, w2c, w2c, voff2, voff2, tr2, tr2, pdst);
}

__global__ NOPK __launch_bounds__(320) void k_mid(const u16* __restrict__ wr, const u16* __restrict__ wi, char* __restrict__ ws) {
  const int lane = TID & 63, wid = TID >> 6;
  const int m1 = BID & 7, kp = (BID >> 3) * 5 + wid;
  if (kp >= 145) return;
  const int g = lane >> 4, c = lane & 15;
  u32x2 d[4];
#pragma unroll
  for (int jj = 0; jj < 4; ++jj) d[jj] = *(const u32x2*)(ws + Z_OFF + ((size_t)(16 * (4 * g + jj) + c) * 8 + m1) * Z_PITCH + kp * 8);
  float fr[2][4], fi[2][4];
#pragma unroll
  for (int e = 0; e < 4; ++e)
#pragma unroll
    for (int t = 0; t < 2; ++t) {
      const int k = (int)umin(2 * kp + t, 288);
      const float sc = ((k == 0 || k == 288) ? 1.f : 2.f) * (64.f / (2048.f * 576.f));
      const size_t o = (size_t)(m1 + 8 * c + 128 * (4 * g + e)) * NKF + k;
      fr[t][e] = bflo(wr[o]) * sc; fi[t][e] = bflo(wi[o]) * sc;
    }
  const u32x4 FR = ldfrag(&T_MID, 0, lane), FI = ldfrag(&T_MID, 1, lane), IR = ldfrag(&T_MID, 2, lane), II = ldfrag(&T_MID, 3, lane);
  const f32x4 t0 = ldf4(&T_MIDT, 0, lane), t1 = ldf4(&T_MIDT, 1, lane);
  const float tc[4] = {t0[0], t0[2], t1[0], t1[2]}, ts[4] = {t0[1], t0[3], t1[1], t1[3]};
  const f32x4 z = {0.f, 0.f, 0.f, 0.f};
  u32 o[2][4];
#pragma unroll
  for (int t = 0; t < 2; ++t) {
    u32x4 a = {d[0][t], d[1][t], d[2][t], d[3][t]};
    f32x4 re = mfma16h(a, FR, z), im = mfma16h(a, FI, z); settle(re, im);
#pragma unroll
    for (int e = 0; e < 4; ++e) a[e] = pkhf(cmulc(re[e], im[e], tc[e], ts[e]));
    re = mfma16h(FR, a, z); im = mfma16h(FI, a, z); settle(re, im);
#pragma unroll
    for (int e = 0; e < 4; ++e) a[e] = pkbf(cmul(re[e], im[e], fr[t][e], fi[t][e]));
    re = mfma16b(a, IR, z); im = mfma16b(a, II, z); settle(re, im);
#pragma unroll
    for (int e = 0; e < 4; ++e) a[e] = pkbf(cmul(re[e], im[e], tc[e], ts[e]));
    re = mfma16b(a, IR, z); im = mfma16b(a, II, z); settle(re, im);
#pragma unroll
    for (int e = 0; e < 4; ++e) o[t][e] = pkbf(re[e], im[e]);
  }
  u32x4* vd = (u32x4*)(ws + V_OFF + ((size_t)m1 * 145 + kp) * 2048 + (16 * c + 4 * g) * 8);
  vd[0] = u32x4{o[0][0], o[1][0], o[0][1], o[1][1]};
  vd[1] = u32x4{o[0][2], o[1][2], o[0][3], o[1][3]};
}

__global__ NOPK __launch_bounds__(512) void k_fin(const u16* __restrict__ x, const u16* __restrict__ b2, const char* __restrict__ ws, u16* __restrict__ out) {
  __shared__ __attribute__((aligned(16))) char smem[8 * 1152 + 64 + 8 * 576 * 4];
  const u32 lds = (u32)(size_t)smem;
  constexpr u32 L_Y = 0, L_NY = 8 * 1152, L_S = L_NY + 64;
  const int tid = TID, lane = tid & 63, wid = tid >> 6;
  const int j = BID & 7, i = BID >> 3, b = 32 * j + i;
  if (tid < NKF) {
    float re[8], im[8];
#pragma unroll
    for (int m = 0; m < 8; ++m) {
      const u32 v = *(const u32*)(ws + V_OFF + ((size_t)m * 145 + (tid >> 1)) * 2048 + b * 8 + (tid & 1) * 4);
      const float c = T_TW8.v[b][2 * m], s = T_TW8.v[b][2 * m + 1], vr = bflo(v), vi = bfhi(v);
      const f32x2 t = cmul(vr, vi, c, s); re[m] = t[0]; im[m] = t[1];
    }
    fft8<1>(re, im);
    const int k2 = tid / 18, k1 = tid - 18 * k2;
#pragma unroll
    for (int a = 0; a < 8; ++a) {
      if (tid < 288) ldsw<u32>(lds + L_Y + a * 1152 + k1 * 64 + k2 * 4, pkbf(re[a], im[a]));
      else ldsw<float>(lds + L_NY + a * 4, re[a]);
    }
  }
  bar_lds();
  {
    const u32 h = lane >> 5, r = lane & 31;
    const u32 ya = lds + L_Y + wid * 1152 + umin(r, 17u) * 64 + h * 16;
    const u32x4 A0 = ldsr<u32x4>(ya), A1 = ldsr<u32x4>(ya + 32);
    f32x16 z; for (int e = 0; e < 16; ++e) z[e] = 0.f;
    f32x16 vr = z;
    const float ny = ldsr<float>(lds + L_NY + wid * 4);
    vr[0] = h ? 0.f : ((lane & 1) ? -ny : ny);
    vr = mfma32b(A0, ldfrag(&T_RI1, 0, lane), vr); vr = mfma32b(A1, ldfrag(&T_RI1, 1, lane), vr);
    f32x16 vi = mfma32b(A0, ldfrag(&T_RI1, 2, lane), z); vi = mfma32b(A1, ldfrag(&T_RI1, 3, lane), vi);
    settle(vr, vi);
    u32 pk[12];
#pragma unroll
    for (int e = 0; e < 5; ++e) {
      const f32x4 tw = ldf4(&T_RIT, e, lane);
      pk[2 * e] = pkbf(cmul(vr[2 * e], vi[2 * e], tw[0], tw[1]));
      pk[2 * e + 1] = pkbf(cmul(vr[2 * e + 1], vi[2 * e + 1], tw[2], tw[3]));
    }
    pk[10] = 0; pk[11] = 0;
    f32x16 y = z;
#pragma unroll
    for (int s = 0; s < 3; ++s) y = mfma32b(ldfrag(&T_RI2, s, lane), u32x4{pk[4 * s], pk[4 * s + 1], pk[4 * s + 2], pk[4 * s + 3]}, y);
    settle(y);
#pragma unroll
    for (int e = 0; e < 10; ++e) {
      const u32 d1 = (e & 3) + 8 * (e >> 2) + 4 * h;
      if (d1 < 18) ldsw<float>(lds + L_S + (wid * 576 + 32 * d1 + r) * 4, rbf(y[e]));
    }
  }
  bar_lds();
  for (int c = tid; c < 576; c += 512) {
    const char* pb = ws + P_OFF + (size_t)((i * 8 + j) * 8) * P_BLK + c * 16;
    u32x4 v[8];
#pragma unroll
    for (int s = 0; s < 8; ++s) v[s] = *(const u32x4*)(pb + s * P_BLK);
    const int n0 = (c >> 2) * 4, rr = c & 3;
    const u32x2 bb = *(const u32x2*)(b2 + n0);
    const float bias[4] = {bflo(bb[0]), bfhi(bb[0]), bflo(bb[1]), bfhi(bb[1])};
#pragma unroll
    for (int t = 0; t < 2; ++t) {
      const int a = 4 * t + rr;
      const size_t go = (size_t)(256 * a + b) * D_ + n0;
      const u32x2 xv = *(const u32x2*)(x + go);
      const float xs[4] = {bflo(xv[0]), bfhi(xv[0]), bflo(xv[1]), bfhi(xv[1])};
      const f32x4 sv = ldsr<f32x4>(lds + L_S + (a * 576 + n0) * 4);
      float acc[4] = {0.f, 0.f, 0.f, 0.f};
#pragma unroll
      for (int s = 0; s < 8; ++s) {
        acc[0] += bflo(v[s][2 * t]); acc[1] += bfhi(v[s][2 * t]); acc[2] += bflo(v[s][2 * t + 1]); acc[3] += bfhi(v[s][2 * t + 1]);
      }
      float res[4];
#pragma unroll
      for (int e = 0; e < 4; ++e) {
        const float o = rbf(rbf(acc[e]) + bias[e]);
        const float gate = rbf(o * __builtin_amdgcn_rcpf(1.f + __builtin_amdgcn_exp2f(-1.4426950408889634f * o)));
        res[e] = xs[e] + rbf(sv[e] * gate);
      }
      *(u32x2*)(out + go) = u32x2{pkbf(res[0], res[1]), pkbf(res[2], res[3])};
    }
  }
}

extern "C" void kernel_launch(void* const* d_in, const int* in_sizes, int n_in, void* d_out, int out_size, void* d_ws, size_t ws_size,
                              hipStream_t stream) {
  const u16* x = (const u16*)d_in[0];
  const u16* wr = (const u16*)d_in[1];
  const u16* wi = (const u16*)d_in[2];
  const u16* w1 = (const u16*)d_in[4];
  const u16* b1 = (const u16*)d_in[5];
  const u16* w2 = (const u16*)d_in[6];
  const u16* b2 = (const u16*)d_in[7];
  (void)hipFuncSetAttribute((const void*)k_main, hipFuncAttributeMaxDynamicSharedMemorySize, L_END);
  k_main<<<256, 512, L_END, stream>>>(x, w1, b1, w2, (char*)d_ws);
  k_mid<<<232, 320, 0, stream>>>(wr, wi, (char*)d_ws);
  k_fin<<<256, 512, 0, stream>>>(x, b2, (const char*)d_ws, (u16*)d_out);
}
```

```cpp
#include <hip/hip_runtime.h>
#include <hip/hip_bf16.h>
#include <stdint.h>
#include <type_traits>

#if defined(__HIP_DEVICE_COMPILE__)
#define NOPK __attribute__((target("no-packed-fp32-ops")))
#else
#define NOPK
#endif
#define DI __device__ __forceinline__ NOPK
#define LAM NOPK __attribute__((always_inline))
#define LAS __attribute__((address_space(3)))
typedef unsigned u32;
typedef unsigned short u16;
typedef u32 u32x2 __attribute__((ext_vector_type(2)));
typedef u32 u32x4 __attribute__((ext_vector_type(4)));
typedef float f32x2 __attribute__((ext_vector_type(2)));
typedef float f32x4 __attribute__((ext_vector_type(4)));
typedef float f32x16 __attribute__((ext_vector_type(16)));
typedef short s16x4 __attribute__((ext_vector_type(4)));
typedef __bf16 bf16x2 __attribute__((ext_vector_type(2)));
typedef __bf16 bf16x8 __attribute__((ext_vector_type(8)));
typedef _Float16 f16x2 __attribute__((ext_vector_type(2)));
typedef _Float16 f16x8 __attribute__((ext_vector_type(8)));

constexpr int S_ = 2048, D_ = 576, F_ = 4608, NKF = 289;

namespace tab {
constexpr double PI = 3.14159265358979323846264338327950288;
constexpr double ccos(int n, int den) {
  if (2 * n > den) n = den - n;
  double sg = 1.0;
  if (4 * n > den) { n = den / 2 - n; sg = -1.0; }
  if (4 * n == den) return 0.0;
  const double x = 2.0 * PI * n / den, x2 = x * x;
  double term = 1.0, sum = 1.0;
  for (int i = 1; i <= 12; ++i) { term *= -x2 / ((2 * i - 1) * (2 * i)); sum += term; }
  return sg * sum;
}
template <int N> struct Cos { float v[N]; };
template <int N> constexpr Cos<N> mkcos() { Cos<N> c{}; for (int i = 0; i < N; ++i) c.v[i] = (float)ccos(i, N); return c; }
constexpr Cos<576> C576 = mkcos<576>();
constexpr Cos<2048> C2048 = mkcos<2048>();
constexpr float c576(int n) { return C576.v[n % 576]; }
constexpr float s576(int n) { return C576.v[(n + 432) % 576]; }
constexpr float c2048(int n) { return C2048.v[n % 2048]; }
constexpr float s2048(int n) { return C2048.v[(n + 1536) % 2048]; }
constexpr u16 tobf(float f) { u32 u = __builtin_bit_cast(u32, f); u += 0x7fffu + ((u >> 16) & 1u); return (u16)(u >> 16); }
constexpr u16 tohf(float f) { return __builtin_bit_cast(u16, (_Float16)f); }
constexpr int crow(int reg, int h) { return (reg & 3) + 8 * (reg >> 2) + 4 * h; }

template <int N> struct alignas(16) Frags { u16 v[N][64][8]; };
template <int N> struct alignas(16) F4s { float v[N][64][4]; };

constexpr Frags<4> mk_rf1() {
  Frags<4> t{};
  for (int f = 0; f < 4; ++f) for (int l = 0; l < 64; ++l) for (int j = 0; j < 8; ++j) {
    const int d1 = 16 * (f & 1) + 8 * (l >> 5) + j, k1 = l & 31;
    float v = 0.f;
    if (d1 < 18 && k1 < 18) v = (f < 2) ? c576(32 * d1 * k1) : -s576(32 * d1 * k1);
    t.v[f][l][j] = tohf(v);
  }
  return t;
}
constexpr F4s<8> mk_rft() {
  F4s<8> t{};
  for (int e = 0; e < 8; ++e) for (int l = 0; l < 64; ++l) for (int q = 0; q < 2; ++q) {
    const int d2 = crow(2 * e + q, l >> 5), k1 = l & 31;
    t.v[e][l][2 * q] = c576(d2 * k1); t.v[e][l][2 * q + 1] = s576(d2 * k1);
  }
  return t;
}
constexpr Frags<4> mk_rf2() {
  Frags<4> t{};
  for (int s = 0; s < 4; ++s) for (int l = 0; l < 64; ++l) for (int j = 0; j < 8; ++j) {
    const int d2 = crow(4 * s + (j >> 1), l >> 5), c = j & 1, k2 = (l & 31) >> 1, co = l & 1;
    const float cs = c576(18 * d2 * k2), sn = s576(18 * d2 * k2);
    t.v[s][l][j] = tohf(co == 0 ? (c == 0 ? cs : sn) : (c == 0 ? -sn : cs));
  }
  return t;
}
constexpr Frags<4> mk_mid() {
  Frags<4> t{};
  for (int l = 0; l < 64; ++l) for (int j = 0; j < 8; ++j) {
    const int idx = 4 * (l >> 4) + (j >> 1), c = j & 1, o = l & 15;
    const float cs = c2048(128 * idx * o), sn = s2048(128 * idx * o);
    t.v[0][l][j] = tohf(c == 0 ? cs : sn);
    t.v[1][l][j] = tohf(c == 0 ? -sn : cs);
    t.v[2][l][j] = tobf(c == 0 ? cs : -sn);
    t.v[3][l][j] = tobf(c == 0 ? sn : cs);
  }
  return t;
}
constexpr F4s<2> mk_midt() {
  F4s<2> t{};
  for (int e = 0; e < 2; ++e) for (int l = 0; l < 64; ++l) for (int q = 0; q < 2; ++q) {
    const int r = 4 * (l >> 4) + 2 * e + q, c = l & 15;
    t.v[e][l][2 * q] = c2048(8 * r * c); t.v[e][l][2 * q + 1] = s2048(8 * r * c);
  }
  return t;
}
constexpr Frags<4> mk_ri1() {
  Frags<4> t{};
  for (int f = 0; f < 4; ++f) for (int l = 0; l < 64; ++l) for (int j = 0; j < 8; ++j) {
    const int k2 = 8 * (f & 1) + 4 * (l >> 5) + (j >> 1), c = j & 1, d2 = l & 31;
    const float cs = c576(18 * d2 * k2), sn = s576(18 * d2 * k2);
    t.v[f][l][j] = tobf(f < 2 ? (c == 0 ? cs : -sn) : (c == 0 ? sn : cs));
  }
  return t;
}
constexpr F4s<5> mk_rit() {
  F4s<5> t{};
  for (int e = 0; e < 5; ++e) for (int l = 0; l < 64; ++l) for (int q = 0; q < 2; ++q) {
    const int k1 = crow(2 * e + q, l >> 5), d2 = l & 31;
    t.v[e][l][2 * q] = c576(d2 * k1); t.v[e][l][2 * q + 1] = s576(d2 * k1);
  }
  return t;
}
constexpr Frags<3> mk_ri2() {
  Frags<3> t{};
  for (int s = 0; s < 3; ++s) for (int l = 0; l < 64; ++l) for (int j = 0; j < 8; ++j) {
    const int reg = 4 * s + (j >> 1), k1 = crow(reg, l >> 5), c = j & 1, d1 = l & 31;
    float v = 0.f;
    if (reg < 10 && k1 < 18 && d1 < 18) v = (c == 0) ? c576(32 * d1 * k1) : -s576(32 * d1 * k1);
    t.v[s][l][j] = tobf(v);
  }
  return t;
}
struct alignas(64) Tw8 { float v[256][16]; };
constexpr Tw8 mk_tw8() {
  Tw8 t{};
  for (int b = 0; b < 256; ++b) for (int m = 0; m < 8; ++m) { t.v[b][2 * m] = c2048(b * m); t.v[b][2 * m + 1] = s2048(b * m); }
  return t;
}
}
namespace tab {
struct alignas(1024) All { Frags<4> rf1; F4s<8> rft; Frags<4> rf2; Frags<4> mid; F4s<2> midt; Frags<4> ri1; F4s<5> rit; Frags<3> ri2; };
constexpr Frags<4> RF1 = mk_rf1(); constexpr F4s<8> RFT = mk_rft(); constexpr Frags<4> RF2 = mk_rf2(); constexpr Frags<4> MID = mk_mid();
constexpr F4s<2> MIDT = mk_midt(); constexpr Frags<4> RI1 = mk_ri1(); constexpr F4s<5> RIT = mk_rit(); constexpr Frags<3> RI2 = mk_ri2();
}
static __device__ const tab::All T_ALL = {tab::RF1, tab::RFT, tab::RF2, tab::MID, tab::MIDT, tab::RI1, tab::RIT, tab::RI2};
static __device__ const tab::Tw8 T_TW8 = tab::mk_tw8();
constexpr int TB_FWD = 0, TB_MID = 16, TB_INV = 22;

constexpr size_t al4k(size_t x) { return (x + 4095) / 4096 * 4096; }
constexpr size_t E_OFF = 0, F2_OFF = 4096, FA_OFF = 8192, FB_OFF = 12288;
constexpr size_t RS_OFF = 16384;
constexpr size_t CS_OFF = RS_OFF + 2048 * 16;
constexpr size_t Z_PITCH = 292 * 4;
constexpr size_t Z_OFF = al4k(CS_OFF + 256 * 2048), Z_BYTES = 256 * 8 * Z_PITCH;
constexpr size_t V_OFF = al4k(Z_OFF + Z_BYTES), V_BYTES = (size_t)8 * 97 * 3072;
constexpr size_t P_OFF = al4k(V_OFF + V_BYTES), P_BLK = 9216, P_BYTES = (size_t)256 * 4 * P_BLK;
constexpr size_t H_OFF = al4k(P_OFF + P_BYTES), H_BYTES = (size_t)16 * 144 * 8 * 1024;

#define ST(n)
#define SC(n)
#define SB() __builtin_amdgcn_sched_barrier(0)
#define WAIT_V(n) asm volatile("s_waitcnt vmcnt(%0)" ::"n"(n) : "memory")
#define WAIT_L0() asm volatile("s_waitcnt lgkmcnt(0)" ::: "memory")
DI void bar() { asm volatile("" ::: "memory"); __builtin_amdgcn_s_barrier(); asm volatile("" ::: "memory"); }
DI void bar_lds() { WAIT_L0(); bar(); }
template <int CTRL> DI float dppf(float v) { return __builtin_bit_cast(float, __builtin_amdgcn_update_dpp(0, __builtin_bit_cast(int, v), CTRL, 0xf, 0xf, true)); }
DI float sum8(float v) { v += dppf<0xB1>(v); v += dppf<0x4E>(v); v += dppf<0x141>(v); return v; }
DI float rdlane(float v, int l) { return __builtin_bit_cast(float, __builtin_amdgcn_readlane(__builtin_bit_cast(int, v), l)); }
DI u32 umin(u32 a, u32 b) { return a < b ? a : b; }
#define TID ((int)__builtin_amdgcn_workitem_id_x())
#define BID ((int)__builtin_amdgcn_workgroup_id_x())
DI float bflo(u32 v) { return __builtin_bit_cast(float, v << 16); }
DI float bfhi(u32 v) { return __builtin_bit_cast(float, v & 0xffff0000u); }
DI u32 pkbf(float a, float b) { bf16x2 v = {(__bf16)a, (__bf16)b}; return __builtin_bit_cast(u32, v); }
DI u32 pkhf(float a, float b) { f16x2 v = {(_Float16)a, (_Float16)b}; return __builtin_bit_cast(u32, v); }
DI float rbf(float a) { return bflo(pkbf(a, a)); }
DI float dot2(u32 a, u32 b, float c) { return __builtin_amdgcn_fdot2_f32_bf16(__builtin_bit_cast(bf16x2, a), __builtin_bit_cast(bf16x2, b), c, false); }
DI f32x4 mfma16b(u32x4 a, u32x4 b, f32x4 c) { return __builtin_amdgcn_mfma_f32_16x16x32_bf16(__builtin_bit_cast(bf16x8, a), __builtin_bit_cast(bf16x8, b), c, 0, 0, 0); }
DI f32x4 mfma16h(u32x4 a, u32x4 b, f32x4 c) { return __builtin_amdgcn_mfma_f32_16x16x32_f16(__builtin_bit_cast(f16x8, a), __builtin_bit_cast(f16x8, b), c, 0, 0, 0); }
DI f32x16 mfma32b(u32x4 a, u32x4 b, f32x16 c) { return __builtin_amdgcn_mfma_f32_32x32x16_bf16(__builtin_bit_cast(bf16x8, a), __builtin_bit_cast(bf16x8, b), c, 0, 0, 0); }
DI f32x16 mfma32h(u32x4 a, u32x4 b, f32x16 c) { return __builtin_amdgcn_mfma_f32_32x32x16_f16(__builtin_bit_cast(f16x8, a), __builtin_bit_cast(f16x8, b), c, 0, 0, 0); }
DI void mma(f32x4& c, u32x4 a, u32x4 b) { asm volatile("v_mfma_f32_16x16x32_bf16 %0, %1, %2, %0" : "+v"(c) : "v"(a), "v"(b)); }
DI void mma0(f32x4& d, u32x4 a, u32x4 b, f32x4 c) { asm volatile("v_mfma_f32_16x16x32_bf16 %0, %1, %2, %3" : "=&v"(d) : "v"(a), "v"(b), "v"(c)); }
DI u32x2 trread(u32 addr) { return __builtin_bit_cast(u32x2, __builtin_amdgcn_ds_read_tr16_b64_v4i16((LAS s16x4*)(size_t)addr)); }
DI u32x4 ldfrag(const void* tbl, int f, int lane) { return ((const u32x4*)tbl)[f * 64 + lane]; }
DI f32x4 ldf4(const void* tbl, int f, int lane) { return ((const f32x4*)tbl)[f * 64 + lane]; }
DI void glds(const char* sbase, u32 voff, u32 dst) {
  asm volatile("s_mov_b32 m0, %2\n\ts_nop 0\n\tglobal_load_lds_dwordx4 %0, %1" ::"v"(voff), "s"(sbase), "s"(dst) : "memory");
}
DI float fmul1(float a, float b) { return a * b; }
DI float ffma1(float a, float b, float c) { return __builtin_fmaf(a, b, c); }
DI float fnma1(float a, float b, float c) { return __builtin_fmaf(-a, b, c); }
DI f32x2 cmul(float x, float y, float c, float s) { return f32x2{fnma1(y, s, fmul1(x, c)), ffma1(x, s, fmul1(y, c))}; }
DI f32x2 cmulc(float x, float y, float c, float s) { return f32x2{ffma1(y, s, fmul1(x, c)), fnma1(x, s, fmul1(y, c))}; }
DI u32 pkbf(f32x2 v) { return pkbf(v[0], v[1]); }
DI u32 pkhf(f32x2 v) { return pkhf(v[0], v[1]); }
DI void glds4(const char* sbase, u32 voff, u32 dst) {
  asm volatile("s_mov_b32 m0, %2\n\ts_nop 0\n\tglobal_load_lds_dword %0, %1" ::"v"(voff), "s"(sbase), "s"(dst) : "memory");
}
DI void settle(f32x16& a, f32x16& b) { asm volatile("s_nop 7\n\ts_nop 7" : "+v"(a), "+v"(b)); }
DI void settle(f32x16& a) { asm volatile("s_nop 7\n\ts_nop 7" : "+v"(a)); }
DI void settle(f32x4& a, f32x4& b) { asm volatile("s_nop 7\n\ts_nop 1" : "+v"(a), "+v"(b)); }
template <typename T> DI T ldsr(u32 addr) { return *(const LAS T*)(size_t)addr; }
template <typename T> DI void ldsw(u32 addr, T v) { *(LAS T*)(size_t)addr = v; }

template <int SGN> DI void fft8(float (&r)[8], float (&i)[8]) {
  constexpr float R = 0.70710678118654752f;
  float ar[8], ai[8];
#pragma unroll
  for (int n = 0; n < 4; ++n) { ar[n] = r[n] + r[n + 4]; ai[n] = i[n] + i[n + 4]; ar[n + 4] = r[n] - r[n + 4]; ai[n + 4] = i[n] - i[n + 4]; }
  { float x = ar[5], y = ai[5]; ar[5] = (x - SGN * y) * R; ai[5] = (y + SGN * x) * R; }
  { float x = ar[6], y = ai[6]; ar[6] = -SGN * y; ai[6] = SGN * x; }
  { float x = ar[7], y = ai[7]; ar[7] = (-x - SGN * y) * R; ai[7] = (-y + SGN * x) * R; }
#pragma unroll
  for (int hlf = 0; hlf < 2; ++hlf) {
    const int o = 4 * hlf;
    const float b0r = ar[o] + ar[o + 2], b0i = ai[o] + ai[o + 2], b2r = ar[o] - ar[o + 2], b2i = ai[o] - ai[o + 2];
    const float b1r = ar[o + 1] + ar[o + 3], b1i = ai[o + 1] + ai[o + 3];
    const float tr = ar[o + 1] - ar[o + 3], ti = ai[o + 1] - ai[o + 3];
    const float b3r = -SGN * ti, b3i = SGN * tr;
    r[hlf] = b0r + b1r; i[hlf] = b0i + b1i;
    r[hlf + 4] = b0r - b1r; i[hlf + 4] = b0i - b1i;
    r[hlf + 2] = b2r + b3r; i[hlf + 2] = b2i + b3i;
    r[hlf + 6] = b2r - b3r; i[hlf + 6] = b2i - b3i;
  }
}

DI float relu1(float v) { const int b = __builtin_bit_cast(int, v); return __builtin_bit_cast(float, b > 0 ? b : 0); }
DI float sum64(float v) {
  v = sum8(v); v += dppf<0x140>(v);
  return (rdlane(v, 0) + rdlane(v, 16)) + (rdlane(v, 32) + rdlane(v, 48));
}
typedef u32 u32x3 __attribute__((ext_vector_type(3)));
DI void gl16(u32x4& d, const void* sb, u32 vo) { asm volatile("global_load_dwordx4 %0, %1, %2" : "=v"(d) : "v"(vo), "s"(sb) : "memory"); }
DI void gl8(u32x2& d, const void* sb, u32 vo) { asm volatile("global_load_dwordx2 %0, %1, %2" : "=v"(d) : "v"(vo), "s"(sb) : "memory"); }
DI void gl4(u32& d, const void* sb, u32 vo) { asm volatile("global_load_dword %0, %1, %2" : "=v"(d) : "v"(vo), "s"(sb) : "memory"); }
DI void gl16c(u32x4& d, const void* sb, u32 vo) { asm volatile("global_load_dwordx4 %0, %1, %2 sc1" : "=v"(d) : "v"(vo), "s"(sb) : "memory"); }
DI void gl12c(u32x3& d, const void* sb, u32 vo) { asm volatile("global_load_dwordx3 %0, %1, %2 sc1" : "=v"(d) : "v"(vo), "s"(sb) : "memory"); }
DI void gl8c(u32x2& d, const void* sb, u32 vo) { asm volatile("global_load_dwordx2 %0, %1, %2 sc1" : "=v"(d) : "v"(vo), "s"(sb) : "memory"); }
DI void gl4c(u32& d, const void* sb, u32 vo) { asm volatile("global_load_dword %0, %1, %2 sc1" : "=v"(d) : "v"(vo), "s"(sb) : "memory"); }
DI void gs16c(void* sb, u32 vo, u32x4 v) { asm volatile("global_store_dwordx4 %0, %1, %2 sc1\n\ts_nop 1" ::"v"(vo), "v"(v), "s"(sb) : "memory"); }
DI void gs8c(void* sb, u32 vo, u32x2 v) { asm volatile("global_store_dwordx2 %0, %1, %2 sc1" ::"v"(vo), "v"(v), "s"(sb) : "memory"); }
DI void gs4c(void* sb, u32 vo, u32 v) { asm volatile("global_store_dword %0, %1, %2 sc1" ::"v"(vo), "v"(v), "s"(sb) : "memory"); }
DI void gs8(void* sb, u32 vo, u32x2 v) { asm volatile("global_store_dwordx2 %0, %1, %2" ::"v"(vo), "v"(v), "s"(sb) : "memory"); }
template <typename T> DI void pin(T& v) { asm volatile("" : "+v"(v)); }
DI bool anylane(bool p) { return __builtin_amdgcn_ballot_w64(p) != 0; }
DI void mmaz(f32x4& d, u32x4 a, u32x4 b) { asm volatile("v_mfma_f32_16x16x32_bf16 %0, %1, %2, 0" : "=&v"(d) : "v"(a), "v"(b)); }
template <int I, int N, typename F> DI void sfor(F&& f) { if constexpr (I < N) { f(std::integral_constant<int, I>{}); sfor<I + 1, N>(f); } }
DI void mma16(f32x4& c, u32x2 a, u32x2 b) { asm volatile("v_mfma_f32_16x16x16_bf16 %0, %1, %2, %0" : "+v"(c) : "v"(a), "v"(b)); }
constexpr int SPIN_CAP = 1 << 15;
#define POLL(BAD, RELOAD, PINS) do { if (__builtin_expect(anylane(BAD), 0)) { int sp_ = 0; \
    do { __builtin_amdgcn_s_sleep(4); RELOAD; WAIT_V(0); PINS; } while (anylane(BAD) && ++sp_ < SPIN_CAP); } } while (0)

constexpr u32 SLOT = 24576, SLOT2 = 18432;
constexpr u32 L_ROWS = 6 * SLOT, ROWP = 1168, L_CB = L_ROWS, L_CZ = L_CB + 2048, L_CA = L_ROWS + 8 * ROWP, L_RI = L_CA + 320 * 16, L_END = L_RI + 32;
constexpr u32 L_Y = 73728, L_NY = L_Y + 8 * 1152, L_S = L_NY + 64;
static_assert(L_END <= 163840, "LDS");

__global__ NOPK __launch_bounds__(512, 2) void k_all(const u16* __restrict__ x, const u16* __restrict__ wr, const u16* __restrict__ wi,
                                                     const u16* __restrict__ w1, const u16* __restrict__ b1, const u16* __restrict__ w2,
                                                     const u16* __restrict__ b2, char* __restrict__ ws, u16* __restrict__ out) {
  extern __shared__ __attribute__((aligned(1024))) char smem[];
  const u32 lds = (u32)(size_t)smem;
  const int tid = TID, lane = tid & 63, wid = __builtin_amdgcn_readfirstlane(tid >> 6);
  const int xcd = BID & 7, idx = BID >> 3, b = 32 * xcd + idx, r_ = idx & 3, i_ = (idx >> 2) + 8 * (xcd >> 2), q_ = xcd & 3, j_ = 4 * q_ + r_;
  const int mg = wid & 3, ng = wid >> 2, odd = r_ & 1, rot = i_ & 3;
  const char* tb = (const char*)&T_ALL;
  const u32 hv = lane * 16;
  const u32 lr = lane >> 3, lc = lane & 7, g = lane >> 4, q = (lane & 15) >> 2, p = lane & 3;
  ST(0);
  u32x2 ep; gl8c(ep, ws + E_OFF, 0u * lane);
  u32 xv[5];
  {
    const char* xr = (const char*)x + (size_t)(256 * wid + b) * 1152;
#pragma unroll
    for (int u = 0; u < 4; ++u) gl4(xv[u], xr + 256 * u, lane * 4);
    gl4(xv[4], xr + 1024, (lane & 31) * 4);
  }
  u32x4 tf[16];
#pragma unroll
  for (int e = 0; e < 16; ++e) gl16(tf[e], tb + (TB_FWD + e) * 1024, hv);
  const int c0 = 288 * j_ - 32 * odd;
  u32 bb; gl4(bb, b1 + c0, umin(tid, 159) * 4);
  const char* w1c = (const char*)w1 + c0 * 2;
  auto slab = [&](int r) LAM { return r == 4 ? (odd ? 0 : 4) : (((r + rot) & 3) + odd); };
  const u32 voffw = (lr + wid * 8) * 9216u + ((((lc >> 1) ^ (((lr >> 1) & 1) | ((wid & 1) << 1))) << 1) | (lc & 1)) * 16;
  const u32 dstw = lds + wid * 1024;
  auto issue_w1 = [&](int n, int u) LAM { glds(w1c + slab(n / 3) * 128 + (size_t)((n % 3) * 192 + u * 64) * 9216, voffw, dstw + (n < 3 ? 2 + 3 * (n & 1) : n % 6) * SLOT + u * 8192); };
  {
    const char* xt = (const char*)x + (size_t)(1024 * (wid & 1) + 32 * (wid >> 1) + 128 * (i_ >> 3) + 4 * (i_ & 7)) * 1152;
    const u32 vx0 = (256 * (lr & 3) + (lr >> 2)) * 1152 + ((lc ^ (lr >> 1)) * 16), vx1 = (256 * (lr & 3) + 2 + (lr >> 2)) * 1152 + ((lc ^ (4 + (lr >> 1))) * 16);
    auto issue_x = [&](int gi) LAM {
#pragma unroll
      for (int u = 0; u < 3; ++u) {
        glds(xt + (gi * 3 + u) * 128, vx0, lds + (gi & 1) * 3 * SLOT + u * 16384 + wid * 2048);
        glds(xt + (gi * 3 + u) * 128, vx1, lds + (gi & 1) * 3 * SLOT + u * 16384 + wid * 2048 + 1024);
      }
    };
    issue_x(0); issue_w1(0, 0); issue_w1(0, 1); issue_w1(0, 2); issue_x(1); issue_w1(1, 0); issue_w1(1, 1); issue_w1(1, 2);

    WAIT_V(35);
    pin(ep); pin(xv[0]); pin(xv[1]); pin(xv[2]); pin(xv[3]); pin(xv[4]);
    ST(1);
    const unsigned long long tg = (((unsigned long long)(u32)__builtin_amdgcn_readfirstlane(ep[1]) << 32) | (u32)__builtin_amdgcn_readfirstlane(ep[0])) + 0x9E3779B97F4A7C15ull;
    const u32 T0 = (u32)tg, T1 = (u32)(tg >> 32), G0 = T0 << 16, G1 = (u32)(tg >> 16);
    auto bad_unit = [&](u32x4 v) LAM { return ((((v[0] ^ G0) | (v[2] ^ G0)) >> 16) | (v[1] ^ G1) | (v[3] ^ G1)) != 0; };
    auto bad_flags = [&](u32x4 v) LAM { return ((v[0] ^ T0) | (v[1] ^ T1) | (v[2] ^ T0) | (v[3] ^ T1)) != 0; };
    if (lane >= 32) xv[4] = 0;
    {
      float sm = 0.f, sq = 0.f;
#pragma unroll
      for (int u = 0; u < 5; ++u) { sm = dot2(xv[u], 0x3f803f80u, sm); sq = dot2(xv[u], xv[u], sq); }
      sm = sum64(sm); sq = sum64(sq);
      const float mean = sm * (1.f / 576.f);
      const float var = __builtin_fmaxf(sq * (1.f / 576.f) - mean * mean, 0.f);
      const float mb = rbf(mean), den = rbf(__builtin_amdgcn_sqrtf(rbf(rbf(var) + 1e-5f)));
      const float inv = __builtin_amdgcn_rcpf(den);
      if (lane == 0) {
        gs16c(ws + RS_OFF + ((i_ * 8 + 2 * q_ + (wid >> 2)) * 16 + 4 * r_ + (wid & 3)) * 16, 0u * lane,
              u32x4{G0 | (__builtin_bit_cast(u32, -mb) >> 16), G1, G0 | (__builtin_bit_cast(u32, den) >> 16), G1});
        ldsw<float>(lds + L_RI + wid * 4, inv * inv);
      }
#pragma unroll
      for (int u = 0; u < 5; ++u) {
        const u32 c = pkbf(bflo(xv[u]) - mb, bfhi(xv[u]) - mb);
        const u32 o = pkbf(bflo(c) * inv, bfhi(c) * inv);
        if (u < 4 || lane < 32) ldsw<u32>(lds + L_ROWS + wid * ROWP + (lane + 64 * u) * 4, pkhf(bflo(o), bfhi(o)));
      }
    }
    WAIT_V(19);
#pragma unroll
    for (int e = 0; e < 16; ++e) pin(tf[e]);
    pin(bb);
    if (tid < 160) { ldsw<u32x2>(lds + L_CA + tid * 32 + 8, u32x2{bb & 0xffffu, 0u}); ldsw<u32x2>(lds + L_CA + tid * 32 + 24, u32x2{bb >> 16, 0u}); }
    {
      const u32 h = lane >> 5, blk = (lane >> 4) & 1;
      const u32 rb = lds + L_ROWS + wid * ROWP + blk * 32 + p * 8;
      const u32x2 a00 = trread(rb + (8 * h + q) * 64), a01 = trread(rb + (8 * h + 4 + q) * 64);
      const u32x2 a10 = trread(rb + umin(16 + 8 * h + q, 17u) * 64);
      const u32x4 A0 = {a00[0], a00[1], a01[0], a01[1]}, A1 = {a10[0], a10[1], a10[0], a10[1]};
      f32x16 z; for (int e = 0; e < 16; ++e) z[e] = 0.f;
      f32x16 tr = mfma32h(A0, tf[0], z), ti = mfma32h(A0, tf[2], z);
      tr = mfma32h(A1, tf[1], tr); ti = mfma32h(A1, tf[3], ti);
      settle(tr, ti);
      float ny = 0.f;
#pragma unroll
      for (int e = 0; e < 16; e += 2) ny += tr[e] - tr[e + 1];
      const float nyq = rdlane(ny, 0) + rdlane(ny, 32);
      u32 pk[16];
#pragma unroll
      for (int e = 0; e < 8; ++e) {
        const f32x4 tw = __builtin_bit_cast(f32x4, tf[4 + e]);
        pk[2 * e] = pkhf(cmulc(tr[2 * e], ti[2 * e], tw[0], tw[1]));
        pk[2 * e + 1] = pkhf(cmulc(tr[2 * e + 1], ti[2 * e + 1], tw[2], tw[3]));
      }
      f32x16 X = z;
#pragma unroll
      for (int s = 0; s < 4; ++s) X = mfma32h(tf[12 + s], u32x4{pk[4 * s], pk[4 * s + 1], pk[4 * s + 2], pk[4 * s + 3]}, X);
      settle(X);
      const u32 k1 = lane & 31;
      if (k1 < 18) {
#pragma unroll
        for (int e = 0; e < 8; ++e) ldsw<u32>(lds + L_ROWS + wid * ROWP + (k1 + 18 * ((e & 1) + 4 * (e >> 1) + 2 * h)) * 4, pkhf(X[2 * e], X[2 * e + 1]));
      }
      if (lane == 0) ldsw<u32>(lds + L_ROWS + wid * ROWP + 288 * 4, pkhf(nyq, 0.f));
    }
    bar_lds();
    ST(2);
    if (tid < NKF) {
      float re[8], im[8];
#pragma unroll
      for (int a = 0; a < 8; ++a) { const f16x2 t = ldsr<f16x2>(lds + L_ROWS + a * ROWP + tid * 4); re[a] = (float)t[0]; im[a] = (float)t[1]; }
      fft8<-1>(re, im);
#pragma unroll
      for (int m = 0; m < 8; ++m) {
        const float c = T_TW8.v[b][2 * m] * (1.f / 64.f), s = T_TW8.v[b][2 * m + 1] * (1.f / 64.f);
        gs4c(ws + Z_OFF + (size_t)(b * 8 + m) * Z_PITCH, tid * 4, pkhf(cmulc(re[m], im[m], c, s)));
      }
    }
    ST(3);

    u32 hvp = hv; pin(hvp);
#define LANES u32 lane = hvp; pin(lane); lane >>= 4; const u32 tid = wid * 64 + lane, g = lane >> 4, q = (lane & 15) >> 2, p = lane & 3; (void)tid; (void)g; (void)q; (void)p
#define TRROW (lds + (8 * g + q) * 128 + p * 8)
#define FSW ((q >> 1) | ((g & 1) << 1))
    u32 tra[3], trb[3];
#pragma unroll
    for (int e = 0; e < 3; ++e) { tra[e] = TRROW + ((2 * ng) ^ FSW) * 32 + 2 * e * SLOT; trb[e] = TRROW + ((2 * ng + 1) ^ FSW) * 32 + 2 * e * SLOT; pin(tra[e]); pin(trb[e]); }
    char* hb = ws + H_OFF + ((size_t)(i_ * 144 + 9 * j_ + ng - odd) * 8 + 2 * mg) * 1024;
    u32 cab = lds + L_CA + (ng * 32 + (lane & 15)) * 16 + (g & 1) * 8, cbb = lds + (g < 2 ? L_CB + mg * 512 + (lane & 15) * 16 + g * 8 : L_CZ);
    pin(cab); pin(cbb);
    u32x4 st[2][18];
    u32x2 wf[4][4];
    f32x4 acc[2][4];
#define RD1(slot, kk, sl, dst) wf[dst][sl] = trread((((sl) >> 1) ? trb[(slot) >> 1] : tra[(slot) >> 1]) + ((slot) & 1) * SLOT + (kk) * 4096 + ((sl) & 1) * 512)
#define FRAG(src, t) u32x4{wf[src][2 * (t)][0], wf[src][2 * (t)][1], wf[src][2 * (t) + 1][0], wf[src][2 * (t) + 1][1]}
    const u32x4 ones = {0x3f803f80u, 0x3f803f80u, 0x3f803f80u, 0x3f803f80u};
    auto put_colsum = [&](f32x4 cs, int sl, int half, bool publish) LAM {
      LANES;
      const float v = p == 0 ? cs[0] : p == 1 ? cs[1] : p == 2 ? cs[2] : cs[3];
      const float hi = rbf(v);
      const u32 w = pkbf(hi, v - hi);
      const u32 col = half * 32 + (mg & 1) * 16 + 4 * g + p;
      if (q == 0) {
        ldsw<u32>(lds + L_CA + (sl * 64 + col) * 16 + (mg >> 1) * 4, w);
        if (publish) gs16c(ws + CS_OFF + (b * 2 + (mg >> 1)) * 1024, col * 16, u32x4{G0 | (w & 0xffffu), G1, G0 | (w >> 16), G1});
      }
    };
    f32x4 cs;
    u32x4 pl;
    {
      const u32 xa0 = lds + 2 * mg * 2048 + (lane & 15) * 128 + ((g ^ ((lane & 15) >> 1)) * 16), xa1 = xa0 ^ 64;
      const u32 csa = TRROW + ((2 * ng + (mg & 1)) ^ FSW) * 32 + (mg >> 1) * 3 * 4096 + 2 * SLOT;
      u32x4 onesv = ones; pin(onesv);
      sfor<0, 3>([&](auto gic) LAM {
        constexpr int gi = decltype(gic)::value, xs = (gi & 1) * 3, wsl = xs + 2;
        SB();
        if (gi == 0) { if (wid < 5) WAIT_V(18); else WAIT_V(10); }
        else if (gi == 1) { if (wid < 5) WAIT_V(15); else WAIT_V(7); }
        else WAIT_V(6);
        bar_lds();
        if (gi == 0) { ST(4); if (tid < 2) ldsw<u32x4>(lds + L_CZ + tid * 256, u32x4{0u, 0u, 0u, 0u}); }
        if (gi == 1) { gl16c(pl, ws + RS_OFF + (i_ * 8 + wid) * 256, (lane & 15) * 16); issue_w1(2, 0); issue_w1(2, 1); issue_w1(2, 2); }
        if (gi == 2) {
          if (tid == 0) gs8c(ws + F2_OFF + b * 8, 0u * lane, u32x2{T0, T1});
          issue_w1(5, 0); issue_w1(5, 1); issue_w1(5, 2);
        }
#pragma unroll
        for (int u = 0; u < 6; ++u)
#pragma unroll
          for (int m = 0; m < 2; ++m) st[m][gi * 6 + u] = ldsr<u32x4>(((u & 1) ? xa1 : xa0) + xs * SLOT + (u >> 1) * 16384 + m * 2048);
#pragma unroll
        for (int kk = 0; kk < 3; ++kk)
#pragma unroll
          for (int sl = 0; sl < 4; ++sl) RD1(wsl, kk, sl, (gi * 6 + kk) % 4);
        u32x2 cf[3][2];
#pragma unroll
        for (int u = 0; u < 3; ++u) { cf[u][0] = trread(csa + xs * SLOT + u * 4096); cf[u][1] = trread(csa + xs * SLOT + u * 4096 + 512); }
        if constexpr (gi == 2) {
          pin(pl);
          POLL(bad_unit(pl), gl16c(pl, ws + RS_OFF + (i_ * 8 + wid) * 256, (lane & 15) * 16), pin(pl));
          const u32 mm = (pl[0] & 0xffffu) * 0x10001u;
          if (lane < 16) ldsw<u32x4>(lds + L_CB + wid * 256 + lane * 16, u32x4{mm, mm, pl[2] & 0xffffu, 0u});
        }
        SB();
        bar_lds();
        if (gi == 0) issue_x(2);
        if (gi == 1) { issue_w1(3, 0); issue_w1(3, 1); issue_w1(3, 2); issue_w1(4, 0); issue_w1(4, 1); issue_w1(4, 2); }
        if (gi == 2) { issue_w1(6, 0); issue_w1(6, 1); issue_w1(6, 2); }
#pragma unroll
        for (int kk = 0; kk < 6; ++kk) {
          const int ks = gi * 6 + kk;
          if (gi == 2 && kk == 3) { SB(); WAIT_V(9); bar(); }
#pragma unroll
          for (int sl = 0; sl < 4; ++sl) {
            const int t = sl >> 1, m = sl & 1;
            SB();
            if (ks == 0) mmaz(acc[0][sl], FRAG(ks % 4, t), st[m][ks]); else mma(acc[0][sl], FRAG(ks % 4, t), st[m][ks]);
            SB();
            if (kk < 3) RD1(wsl, kk + 3, sl, (ks + 3) % 4);
            else if (gi == 2) RD1(3, kk - 3, sl, (ks + 3) % 4);
            SB();
            if (kk < 3 && sl == 3) { const u32x4 a = {cf[kk][0][0], cf[kk][0][1], cf[kk][1][0], cf[kk][1][1]}; if (ks == 0) mmaz(cs, a, onesv); else mma(cs, a, onesv); }
            if (gi == 2 && kk == 4 && sl == 1) issue_w1(7, 0);
          }
        }
      });
    }
    ST(5);
    SB();
    put_colsum(cs, slab(0), ng, true);
    SB();
    const char* w2c = (const char*)w2 + (size_t)q_ * 1152 * 1152 + r_ * 288;
    const char* hc = ws + H_OFF + ((size_t)(i_ * 144 + q_ * 36 + ng) * 8 + 2 * mg) * 1024;
    auto sig = [&](int n) LAM {
      if (n >= 16) return 9 * ((r_ >> 1) ^ (n - 16)) + 4;
      const int c = n < 4 ? r_ : r_ ^ ((n - 4) % 3 + 1), rho = n < 4 ? n : (n - 4) / 3;
      return 9 * (c >> 1) + 5 * (c & 1) + ((rho + rot) & 3);
    };
    u32 voffa, voffb;
    auto mk_voff2 = [&]() LAM {
      LANES;
      const u32 ca = wid * 64 + lane, cb = 2048 + (wid & 3) * 64 + lane;
      voffa = (ca / 18) * 1152 + (ca % 18) * 16; voffb = (cb / 72 + (wid >> 2) * 32) * 1152 + (cb % 72) * 4;
    };
    auto issue_w2 = [&](int n, int u) LAM {
      const char* src = w2c + (size_t)sig(n) * 64 * 1152;
      const u32 d = lds + ((n + 4) & 7) * SLOT2;
      if (u == 0) glds(src, voffa, d + wid * 1024);
      if (u == 1) glds(src + 32 * 1152, voffa, d + 9216 + wid * 1024);
      if (u == 2) glds4(src, voffb, d + (wid >> 2) * 9216 + 8192 + (wid & 3) * 256);
    };
    u32x4 hf[6][2];
    auto issue_h = [&](int n, int m) LAM { gl16c(hf[n % 6][m], hc + (size_t)sig(n) * 16384 + m * 1024, hvp); };
    const char* mates = ws + (128 * (i_ >> 3) + 4 * (i_ & 7)) * 8;
    u32x4 fl;
    auto round = [&](auto rc) LAM {
      constexpr int r = decltype(rc)::value, P = r & 1, C0 = r >= 3 ? 0 : 4;
      char* hdst = hb + slab(r - 1) * 16384;
      const u32 cao = slab(r - 1) * 1024;
      float y0, y1;
      u32 pk[8];
      u32x2 ca[2], cb[2]; u32x4 ff;
      sfor<0, 18>([&](auto ksc) LAM {
        constexpr int ks = decltype(ksc)::value, kr = ks + 3, c = 18 * r + ks;
        if constexpr (kr % 6 == 0 && 3 * r + kr / 6 <= 12) {
          SB();
          if constexpr (r == 2 && ks == 3) {
            WAIT_V(5);
            pin(pl);
            LANES;
            const int src = (b & ~12) | ((((int)umin(tid >> 7, 2) + 1 + rot) & 3) << 2);
            POLL(bad_unit(pl), gl16c(pl, ws + CS_OFF, src * 2048 + (tid & 127) * 16), pin(pl));
            ldsw<u32>(lds + L_CA + (((((int)umin(tid >> 7, 2) + 1 + rot) & 3) + odd) * 64 + (tid & 63)) * 16 + ((tid >> 6) & 1) * 4, (pl[0] & 0xffffu) | (pl[2] << 16));
            WAIT_L0();
          } else {
            constexpr int W[13] = {0, 0, 0, 0, 15, 15, 15, 0, 12, 12, 11, 11, 2};
            WAIT_V(W[3 * r + kr / 6]);
            if (r == 1 && ks == 3) asm volatile("s_waitcnt lgkmcnt(8)" ::: "memory");
          }
          bar();
        }
#pragma unroll
        for (int sl = 0; sl < 4; ++sl) {
          const int t = sl >> 1, m = sl & 1, n = ks * 4 + sl, ne = n - 4 * C0;
          SB();
          if (ks == 0) mmaz(acc[P][sl], FRAG(c % 4, t), st[m][ks]); else mma(acc[P][sl], FRAG(c % 4, t), st[m][ks]);
          SB();
          { const int kq = sl == 0 ? kr - 1 : kr, sq = (sl + 3) & 3;
            if ((r < 4 || kq < 18) && !(r == 1 && ks == 0 && sl == 0)) RD1((3 * r + kq / 6) % 6, kq % 6, sq, (18 * r + kq) % 4); }
          SB();
          if (ne == 0) ca[0] = ldsr<u32x2>(cab + cao);
          if (ne == 1) cb[0] = ldsr<u32x2>(cbb);
          if (ne == 2) cb[1] = ldsr<u32x2>(cbb + 256);
          if (ne == 3) ca[1] = ldsr<u32x2>(cab + cao + 256);
          if (ne >= 8 && ne < 12) mma16(acc[1 - P][ne - 8], ca[(ne - 8) >> 1], cb[(ne - 8) & 1]);
          if (ne >= 14 && ne < 46) {
            const int e = ne - 14, pr = e >> 2, tl = pr >> 1, hh = pr & 1;
            if ((e & 3) == 0) asm volatile("v_mul_f32 %0, %1, abs(%1)" : "=v"(y0) : "v"(acc[1 - P][tl][2 * hh]));
            if ((e & 3) == 1) asm volatile("v_mul_f32 %0, %1, abs(%1)" : "=v"(y1) : "v"(acc[1 - P][tl][2 * hh + 1]));
            if ((e & 3) == 2) asm volatile("v_cvt_pk_bf16_f32 %0, %1, %2" : "=v"(pk[pr]) : "v"(y0), "v"(y1));
            if ((e & 3) == 3) asm volatile("v_pk_max_i16 %0, %0, 0" : "+v"(pk[pr]));
          }
          if (ne == 46 || ne == 47) { const int mm = ne - 46; gs16c(hdst + mm * 1024, hvp, u32x4{pk[2 * mm], pk[2 * mm + 1], pk[4 + 2 * mm], pk[4 + 2 * mm + 1]}); }
          if (r == 1 && n == 61) { LANES; const int src = (b & ~12) | ((((int)umin(tid >> 7, 2) + 1 + rot) & 3) << 2); gl16c(pl, ws + CS_OFF, src * 2048 + (tid & 127) * 16); }
          if (sl == 1 && (ks % 2) == 0) {
            const int w = ks + 2, nq = 3 * r + w / 6 + 4;
            if (nq <= 14) issue_w1(nq, (w % 6) / 2);
          }
          if (r == 1 && n < 16) {
            char* ib = ws + P_OFF + (size_t)(i_ * 16 + j_) * 4 * P_BLK;
            if (n == 2) { ff = u32x4{~0u, ~0u, ~0u, ~0u}; pin(ff); }
            if (n == 3 || n == 6 || n == 7 || n == 10) gs16c(ib + wid * 4096 + (n == 3 ? 0 : n == 6 ? 1 : n == 7 ? 2 : 3) * 1024, hvp, ff);
            if (n == 11) { LANES; gs8c(ib + 32768 + wid * 512, lane * 8, u32x2{ff[0], ff[1]}); }
          }
          if (r == 4) {
            if (sl == 3 && ks < 12) issue_w2(ks / 3, ks % 3);
            if (sl == 2 && ks >= 8 && ks < 14) issue_h((ks - 8) >> 1, ks & 1);
            if (n == 60) gl16c(fl, mates + FA_OFF + q_ * 256, hvp & 16);
          }
        }
      });
      SB();
    };
    round(std::integral_constant<int, 1>{});
    round(std::integral_constant<int, 2>{});
    round(std::integral_constant<int, 3>{});
    ST(6);
    auto last_epi = [&](f32x4 (&fa)[4], int sl) LAM {
      const u32x2 ca0 = ldsr<u32x2>(cab + sl * 1024), ca1 = ldsr<u32x2>(cab + sl * 1024 + 256), cb0 = ldsr<u32x2>(cbb), cb1 = ldsr<u32x2>(cbb + 256);
      asm volatile("s_nop 5");
      mma16(fa[0], ca0, cb0); mma16(fa[1], ca0, cb1); mma16(fa[2], ca1, cb0); mma16(fa[3], ca1, cb1);
      asm volatile("s_nop 7\n\ts_nop 1" : "+v"(fa[0]), "+v"(fa[1]), "+v"(fa[2]), "+v"(fa[3]));
#pragma unroll
      for (int m = 0; m < 2; ++m) {
        u32x4 o;
#pragma unroll
        for (int t = 0; t < 2; ++t) {
          f32x4 a = fa[2 * t + m];
#pragma unroll
          for (int e = 0; e < 4; ++e) a[e] = relu1(a[e]);
          o[2 * t] = pkbf(a[0] * a[0], a[1] * a[1]); o[2 * t + 1] = pkbf(a[2] * a[2], a[3] * a[3]);
        }
        gs16c(hb + sl * 16384 + m * 1024, hvp, o);
      }
    };
    mk_voff2();
    WAIT_V(0);
    bar();
    if (wid == 0 && hvp == 0) gs8c(ws + FA_OFF + b * 8, hvp, u32x2{T0, T1});
    if (ng == odd) {
      round(std::integral_constant<int, 4>{});
      WAIT_V(0);
      bar();
      last_epi(acc[0], slab(4));
    }
    int idle = ng ^ odd;
    asm volatile("" : "+s"(idle));
    if (idle) {
      LANES;
      const int task = mg * 32 + idx, m1 = xcd, k0 = 3 * task, kb = k0 < 285 ? k0 : 285, cc = lane & 15;
      u32x4 f0, f1, tm[6];
      u32x2 fr[4], fi[4];
      gl16c(f0, ws + F2_OFF, lane * 32); gl16c(f1, ws + F2_OFF + 16, lane * 32);
#pragma unroll
      for (int e = 0; e < 4; ++e) {
        const u32 o = ((8 * cc + 512 * g) * NKF) * 2;
        gl8(fr[e], wr + (m1 + 128 * e) * NKF + kb, o); gl8(fi[e], wi + (m1 + 128 * e) * NKF + kb, o);
      }
#pragma unroll
      for (int e = 0; e < 6; ++e) gl16(tm[e], tb + (TB_MID + e) * 1024, hvp);
#pragma unroll
      for (int e = 0; e < 12; ++e) issue_w2(e / 3, e % 3);
#pragma unroll
      for (int e = 0; e < 6; ++e) issue_h(e >> 1, e & 1);
      last_epi(acc[1], slab(3));
      WAIT_V(34);
      pin(f0); pin(f1);
      POLL(bad_flags(f0) || bad_flags(f1), gl16c(f0, ws + F2_OFF, lane * 32); gl16c(f1, ws + F2_OFF + 16, lane * 32), pin(f0); pin(f1));
      if (b == 0 && mg == 0 && lane == 0) gs8c(ws + E_OFF, 0u * lane, u32x2{T0, T1});
      u32x3 d[4];
      const int tk = task < 97 ? task : 96;
#pragma unroll
      for (int jj = 0; jj < 4; ++jj) gl12c(d[jj], ws + Z_OFF + (size_t)(jj * 128 + m1) * Z_PITCH + tk * 12, (64 * g + cc) * 8 * (u32)Z_PITCH);
      gl16c(fl, mates + FA_OFF + q_ * 256, (lane & 1) * 16);
      {
        const u32 csa = TRROW + ((2 * odd + (mg & 1)) ^ FSW) * 32 + (mg >> 1) * 3 * 4096;
        u32x4 onesv = ones; pin(onesv);
        f32x4 c2;
#pragma unroll
        for (int e = 0; e < 9; ++e) {
          const u32x2 lo = trread(csa + (e / 3) * SLOT + (e % 3) * 4096), hi = trread(csa + (e / 3) * SLOT + (e % 3) * 4096 + 512);
          const u32x4 a = {lo[0], lo[1], hi[0], hi[1]};
          if (e == 0) mmaz(c2, a, onesv); else mma(c2, a, onesv);
        }
        asm volatile("s_nop 7\n\ts_nop 1" : "+v"(c2));
        put_colsum(c2, slab(4), odd, false);
      }
      WAIT_V(0);
#pragma unroll
      for (int e = 0; e < 4; ++e) { pin(d[e]); pin(fr[e]); pin(fi[e]); }
#pragma unroll
      for (int e = 0; e < 6; ++e) pin(tm[e]);
      if (task < 97) {
        const f32x4 t0 = __builtin_bit_cast(f32x4, tm[4]), t1 = __builtin_bit_cast(f32x4, tm[5]);
        const float tc[4] = {t0[0], t0[2], t1[0], t1[2]}, ts[4] = {t0[1], t0[3], t1[1], t1[3]};
        const f32x4 z = {0.f, 0.f, 0.f, 0.f};
        if (k0 != kb) {
#pragma unroll
          for (int e = 0; e < 4; ++e) { fr[e][0] = fr[e][1] >> 16; fi[e][0] = fi[e][1] >> 16; }
        }
        u32x4 a[3];
        f32x4 re[3], im[3];
#define STAGE(FN, X, Y, XI, YI) _Pragma("unroll") for (int t = 0; t < 3; ++t) { re[t] = FN(X, Y, z); im[t] = FN(XI, YI, z); } \
        asm volatile("s_nop 7\n\ts_nop 1" : "+v"(re[0]), "+v"(im[0]), "+v"(re[1]), "+v"(im[1]), "+v"(re[2]), "+v"(im[2]));
#pragma unroll
        for (int t = 0; t < 3; ++t) a[t] = u32x4{d[0][t], d[1][t], d[2][t], d[3][t]};
        STAGE(mfma16h, a[t], tm[0], a[t], tm[1])
#pragma unroll
        for (int t = 0; t < 3; ++t)
#pragma unroll
          for (int e = 0; e < 4; ++e) a[t][e] = pkhf(cmulc(re[t][e], im[t][e], tc[e], ts[e]));
        STAGE(mfma16h, tm[0], a[t], tm[1], a[t])
#pragma unroll
        for (int t = 0; t < 3; ++t)
#pragma unroll
          for (int e = 0; e < 4; ++e) {
            const float wre = t == 1 ? bfhi(fr[e][0]) : bflo(fr[e][t >> 1]), wim = t == 1 ? bfhi(fi[e][0]) : bflo(fi[e][t >> 1]);
            a[t][e] = pkbf(cmul(re[t][e], im[t][e], wre, wim));
          }
        STAGE(mfma16b, a[t], tm[2], a[t], tm[3])
#pragma unroll
        for (int t = 0; t < 3; ++t)
#pragma unroll
          for (int e = 0; e < 4; ++e) a[t][e] = pkbf(cmul(re[t][e], im[t][e], tc[e], ts[e]));
        STAGE(mfma16b, a[t], tm[2], a[t], tm[3])
#undef STAGE
        u32 o[12];
#pragma unroll
        for (int e = 0; e < 4; ++e)
#pragma unroll
          for (int t = 0; t < 3; ++t) o[3 * e + t] = pkbf(re[t][e], im[t][e]);
#pragma unroll
        for (int e = 0; e < 3; ++e) gs16c(ws + V_OFF + (size_t)(m1 * 97 + task) * 3072 + e * 16, (16 * cc + 4 * g) * 12, u32x4{o[4 * e], o[4 * e + 1], o[4 * e + 2], o[4 * e + 3]});
      }
      bar_lds();
    }
#undef RD1
#undef FRAG
    ST(7);
    {
    LANES;
    u32 tr2[3];
#pragma unroll
    for (int e = 0; e < 3; ++e) { tr2[e] = lds + (32 * ng + 4 * g + q) * 288 + p * 8 + e * 3 * SLOT2; pin(tr2[e]); }
    f32x4 ac[2][9];
    constexpr int PF = 5, NB = 6;
    u32x2 w2f[NB][2];
#define RD2(f, hh) w2f[(f) % NB][hh] = trread(tr2[(((f) / 9 + 4) & 7) / 3] + ((((f) / 9 + 4) & 7) % 3) * SLOT2 + ((f) % 9) * 32 + (hh) * 16 * 288)
#pragma unroll
    for (int f = 0; f < PF; ++f) { RD2(f, 0); RD2(f, 1); }
    pin(fl);
    POLL(bad_flags(fl), gl16c(fl, mates + FA_OFF + q_ * 256, (lane & 1) * 16), pin(fl));
    u32x4 f0, f1, ti_[12];
    u32 vv[8];
    u32x2 xo[2][2], bo[2];
    const u32 kk = umin(tid, 288), voffv = (kk / 3) * 3072 + (kk % 3) * 4;
    sfor<0, 162>([&](auto fc) LAM {
      {
        constexpr int f = decltype(fc)::value, n = f / 9, t = f % 9, fr = f + PF;
        if (t == 0) { pin(hf[n % 6][0]); pin(hf[n % 6][1]); }
        if constexpr (fr % 9 == 0 && n < 17) {
          SB();
          constexpr int W[17] = {40, 40, 20, 18, 16, 18, 18, 19, 10, 19, 18, 18, 17, 4, 21, 22, 26};
          WAIT_V(W[n]);
          bar();
          if (n == 2 && tid == 0) gs8c(ws + FB_OFF + b * 8, 0u * lane, u32x2{T0, T1});
          if constexpr (n == 8) { pin(fl); POLL(bad_flags(fl), gl16c(fl, mates + FB_OFF, ((lane >> 1) & 3) * 256 + (lane & 1) * 16), pin(fl)); }
          if constexpr (n == 13) { pin(f0); pin(f1); POLL(bad_flags(f0) || bad_flags(f1), gl16c(f0, ws + FB_OFF, lane * 32); gl16c(f1, ws + FB_OFF + 16, lane * 32), pin(f0); pin(f1)); }
        }
        const u32x4 a = {w2f[f % NB][0][0], w2f[f % NB][0][1], w2f[f % NB][1][0], w2f[f % NB][1][1]};
        SB();
        if (n == 0) mmaz(ac[0][t], a, hf[0][0]); else mma(ac[0][t], a, hf[n % 6][0]);
        SB();
        if (fr < 162) RD2(fr, 0);
        SB();
        if (n == 0 && t < 4) issue_h(3 + (t >> 1), t & 1);
        if (n == 0 && t >= 4 && t < 7) issue_w2(4, t - 4);
        if (n == 1 && t < 3) issue_w2(5, t);
        if (n == 6 && t == 5) gl16c(fl, mates + FB_OFF, ((lane >> 1) & 3) * 256 + (lane & 1) * 16);
        if (n == 11 && t == 5) { gl16c(f0, ws + FB_OFF, lane * 32); gl16c(f1, ws + FB_OFF + 16, lane * 32); }
        if constexpr (n == 13 && t >= 5) { gl4c(vv[2 * (t - 5)], ws + V_OFF + (size_t)(2 * (t - 5)) * 97 * 3072 + b * 12, voffv); }
        if constexpr (n == 14 || (n == 15 && t < 3)) gl16(ti_[(n - 14) * 9 + t], tb + (TB_INV + (n - 14) * 9 + t) * 1024, hvp);
        if constexpr (n == 15 && t >= 5) { const int e = t - 5; gl8(xo[e >> 1][e & 1], (const char*)x + (size_t)(1024 * (e & 1) + b) * 1152 + (e >> 1) * 1024, (e >> 1) ? (lane & 3) * 256 * 1152 + (lane >> 2) * 8 : (tid & 3) * 256 * 1152 + (tid >> 2) * 8); }
        if constexpr (n == 16 && t < 2) gl8(bo[t], (const char*)b2 + t * 1024, t ? (lane >> 2) * 8 : (tid >> 2) * 8);
        SB();
        if (n == 0) mmaz(ac[1][t], a, hf[0][1]); else mma(ac[1][t], a, hf[n % 6][1]);
        SB();
        if (fr < 162) RD2(fr, 1);
        SB();
        if (n + 6 < 18 && t < 3) issue_w2(n + 6, t);
        if (n + 5 < 18 && (t == 6 || t == 8)) issue_h(n + 5, (t - 6) >> 1);
        if constexpr (n == 13 && t >= 5) { gl4c(vv[2 * (t - 5) + 1], ws + V_OFF + (size_t)(2 * (t - 5) + 1) * 97 * 3072 + b * 12, voffv); }
      }
    });
    SB();
#undef RD2
    ST(8);
    asm volatile("s_nop 7\n\ts_nop 1" ::: "memory");
    bar_lds();
    SB();
    {
      const u32 xb = lds + mg * 18 * 1024 + lane * 16;
      char* pd = ws + P_OFF + (size_t)((i_ * 16 + 4 * mg) * 4 + q_) * P_BLK + r_ * 9 * 256;
      const u32 pv = q * 4 * P_BLK + (g * 4 + p) * 16;
      auto fin = [&](int t) LAM {
        const f32x4 a = ac[0][t] + ldsr<f32x4>(xb + t * 1024), c = ac[1][t] + ldsr<f32x4>(xb + (9 + t) * 1024);
        gs16c(pd + t * 256, pv, u32x4{pkbf(a[0], a[1]), pkbf(a[2], a[3]), pkbf(c[0], c[1]), pkbf(c[2], c[3])});
      };
      if (ng == 0) {
#pragma unroll
        for (int t = 5; t < 9; ++t) { ldsw<f32x4>(xb + t * 1024, ac[0][t]); ldsw<f32x4>(xb + (9 + t) * 1024, ac[1][t]); }
        bar_lds();
#pragma unroll
        for (int t = 0; t < 5; ++t) fin(t);
        WAIT_V(5);
      } else {
#pragma unroll
        for (int t = 0; t < 5; ++t) { ldsw<f32x4>(xb + t * 1024, ac[0][t]); ldsw<f32x4>(xb + (9 + t) * 1024, ac[1][t]); }
        bar_lds();
#pragma unroll
        for (int t = 5; t < 9; ++t) fin(t);
        WAIT_V(4);
      }
    }
    ST(9);
#pragma unroll
    for (int e = 0; e < 8; ++e) pin(vv[e]);
#pragma unroll
    for (int e = 0; e < 12; ++e) pin(ti_[e]);
    pin(xo[0][0]); pin(xo[0][1]); pin(xo[1][0]); pin(xo[1][1]); pin(bo[0]); pin(bo[1]);
    if (tid < NKF) {
      float re[8], im[8];
      const float sc = ((tid == 0 || tid == 288) ? 1.f : 2.f) * (64.f / (2048.f * 576.f));
#pragma unroll
      for (int m = 0; m < 8; ++m) { const f32x2 t = cmul(bflo(vv[m]) * sc, bfhi(vv[m]) * sc, T_TW8.v[b][2 * m], T_TW8.v[b][2 * m + 1]); re[m] = t[0]; im[m] = t[1]; }
      fft8<1>(re, im);
      const int k2 = tid / 18, k1 = tid - 18 * k2;
#pragma unroll
      for (int a = 0; a < 8; ++a) {
        if (tid < 288) ldsw<u32>(lds + L_Y + a * 1152 + k1 * 64 + k2 * 4, pkbf(re[a], im[a]));
        else ldsw<float>(lds + L_NY + a * 4, re[a]);
      }
    }
    bar_lds();
    const char* ib = ws + P_OFF + (size_t)(i_ * 16 + j_) * 4 * P_BLK;
    u32x4 pa[4], pb[4];
    {
      const u32 h = lane >> 5, rr = lane & 31;
      const u32 ya = lds + L_Y + wid * 1152 + umin(rr, 17u) * 64 + h * 16;
      const u32x4 A0 = ldsr<u32x4>(ya), A1 = ldsr<u32x4>(ya + 32);
      f32x16 z; for (int e = 0; e < 16; ++e) z[e] = 0.f;
      f32x16 vr = z;
      const float ny = ldsr<float>(lds + L_NY + wid * 4);
      vr[0] = h ? 0.f : ((lane & 1) ? -ny : ny);
      vr = mfma32b(A0, ti_[0], vr); f32x16 vi = mfma32b(A0, ti_[2], z);
      vr = mfma32b(A1, ti_[1], vr); vi = mfma32b(A1, ti_[3], vi);
      settle(vr, vi);
#pragma unroll
      for (int s = 0; s < 4; ++s) gl16c(pa[s], ib + s * P_BLK, tid * 16);
      if (wid == 7) {
#pragma unroll
        for (int s = 0; s < 4; ++s) gl16c(pb[s], ib + s * P_BLK + 8192, hvp);
      }
      u32 pk[12];
#pragma unroll
      for (int e = 0; e < 5; ++e) {
        const f32x4 tw = __builtin_bit_cast(f32x4, ti_[4 + e]);
        pk[2 * e] = pkbf(cmul(vr[2 * e], vi[2 * e], tw[0], tw[1]));
        pk[2 * e + 1] = pkbf(cmul(vr[2 * e + 1], vi[2 * e + 1], tw[2], tw[3]));
      }
      pk[10] = 0; pk[11] = 0;
      f32x16 y = z;
#pragma unroll
      for (int s = 0; s < 3; ++s) y = mfma32b(ti_[9 + s], u32x4{pk[4 * s], pk[4 * s + 1], pk[4 * s + 2], pk[4 * s + 3]}, y);
      settle(y);
#pragma unroll
      for (int e = 0; e < 10; ++e) {
        const u32 d1 = (e & 3) + 8 * (e >> 2) + 4 * h;
        if (d1 < 18) ldsw<float>(lds + L_S + (wid * 576 + 32 * d1 + rr) * 4, rbf(y[e]));
      }
    }
    bar_lds();
    ST(10);
    auto empty = [&](u32x4 (&v)[4]) LAM {
      u32 mx = 0;
#pragma unroll
      for (int s = 0; s < 4; ++s)
#pragma unroll
        for (int e = 0; e < 4; ++e) mx = v[s][e] > mx ? v[s][e] : mx;
      return mx == ~0u;
    };
    auto finish = [&](u32x4 (&v)[4], int c, u32x2 (&xs2)[2], u32x2 bv) LAM {
      const int n0 = (c >> 2) * 4, rr = c & 3;
      const float bias[4] = {bflo(bv[0]), bfhi(bv[0]), bflo(bv[1]), bfhi(bv[1])};
#pragma unroll
      for (int t = 0; t < 2; ++t) {
        const int a = 4 * t + rr;
        const float xs[4] = {bflo(xs2[t][0]), bfhi(xs2[t][0]), bflo(xs2[t][1]), bfhi(xs2[t][1])};
        const f32x4 sv = ldsr<f32x4>(lds + L_S + (a * 576 + n0) * 4);
        const float i2 = ldsr<float>(lds + L_RI + a * 4);
        float res[4];
#pragma unroll
        for (int e = 0; e < 4; ++e) {
          float sum = 0.f;
#pragma unroll
          for (int s = 0; s < 4; ++s) sum += (e & 1) ? bfhi(v[s][2 * t + (e >> 1)]) : bflo(v[s][2 * t + (e >> 1)]);
          const float o = rbf(rbf(sum * i2) + bias[e]);
          const float gate = rbf(o * __builtin_amdgcn_rcpf(1.f + __builtin_amdgcn_exp2f(-1.4426950408889634f * o)));
          res[e] = xs[e] + rbf(sv[e] * gate);
        }
        gs8((char*)out + (size_t)(1024 * t + b) * 1152, rr * 256 * 1152 + n0 * 2, u32x2{pkbf(res[0], res[1]), pkbf(res[2], res[3])});
      }
    };
    WAIT_V(0);
#define PIN4(v) pin(v[0]); pin(v[1]); pin(v[2]); pin(v[3])
    PIN4(pa);
    POLL(empty(pa), for (int s = 0; s < 4; ++s) gl16c(pa[s], ib + s * P_BLK, tid * 16), PIN4(pa));
    ST(11);
    finish(pa, tid, xo[0], bo[0]);
    if (wid == 7) {
      PIN4(pb);
      POLL(empty(pb), for (int s = 0; s < 4; ++s) gl16c(pb[s], ib + s * P_BLK + 8192, hvp), PIN4(pb));
      finish(pb, 512 + lane, xo[1], bo[1]);
    }
#undef PIN4
    ST(12);
    }
  }
}

extern "C" void kernel_launch(void* const* d_in, const int* in_sizes, int n_in, void* d_out, int out_size, void* d_ws, size_t ws_size,
                              hipStream_t stream) {
  (void)hipFuncSetAttribute((const void*)k_all, hipFuncAttributeMaxDynamicSharedMemorySize, L_END);
  k_all<<<256, 512, L_END, stream>>>((const u16*)d_in[0], (const u16*)d_in[1], (const u16*)d_in[2], (const u16*)d_in[4], (const u16*)d_in[5],
                                     (const u16*)d_in[6], (const u16*)d_in[7], (char*)d_ws, (u16*)d_out);
}
```

```cpp
#include <hip/hip_runtime.h>
#include <hip/hip_bf16.h>
#include <stdint.h>
#include <type_traits>

#if defined(__HIP_DEVICE_COMPILE__)
#define NOPK __attribute__((target("no-packed-fp32-ops")))
#else
#define NOPK
#endif
#define DI __device__ __forceinline__ NOPK
#define LAM NOPK __attribute__((always_inline))
#define LAS __attribute__((address_space(3)))
typedef unsigned u32;
typedef unsigned short u16;
typedef u32 u32x2 __attribute__((ext_vector_type(2)));
typedef u32 u32x4 __attribute__((ext_vector_type(4)));
typedef float f32x2 __attribute__((ext_vector_type(2)));
typedef float f32x4 __attribute__((ext_vector_type(4)));
typedef float f32x16 __attribute__((ext_vector_type(16)));
typedef short s16x4 __attribute__((ext_vector_type(4)));
typedef __bf16 bf16x2 __attribute__((ext_vector_type(2)));
typedef __bf16 bf16x8 __attribute__((ext_vector_type(8)));
typedef _Float16 f16x2 __attribute__((ext_vector_type(2)));
typedef _Float16 f16x8 __attribute__((ext_vector_type(8)));

constexpr int S_ = 2048, D_ = 576, F_ = 4608, NKF = 289;

namespace tab {
constexpr double PI = 3.14159265358979323846264338327950288;
constexpr double ccos(int n, int den) {
  if (2 * n > den) n = den - n;
  double sg = 1.0;
  if (4 * n > den) { n = den / 2 - n; sg = -1.0; }
  if (4 * n == den) return 0.0;
  const double x = 2.0 * PI * n / den, x2 = x * x;
  double term = 1.0, sum = 1.0;
  for (int i = 1; i <= 12; ++i) { term *= -x2 / ((2 * i - 1) * (2 * i)); sum += term; }
  return sg * sum;
}
template <int N> struct Cos { float v[N]; };
template <int N> constexpr Cos<N> mkcos() { Cos<N> c{}; for (int i = 0; i < N; ++i) c.v[i] = (float)ccos(i, N); return c; }
constexpr Cos<576> C576 = mkcos<576>();
constexpr Cos<2048> C2048 = mkcos<2048>();
constexpr float c576(int n) { return C576.v[n % 576]; }
constexpr float s576(int n) { return C576.v[(n + 432) % 576]; }
constexpr float c2048(int n) { return C2048.v[n % 2048]; }
constexpr float s2048(int n) { return C2048.v[(n + 1536) % 2048]; }
constexpr u16 tobf(float f) { u32 u = __builtin_bit_cast(u32, f); u += 0x7fffu + ((u >> 16) & 1u); return (u16)(u >> 16); }
constexpr u16 tohf(float f) { return __builtin_bit_cast(u16, (_Float16)f); }
constexpr int crow(int reg, int h) { return (reg & 3) + 8 * (reg >> 2) + 4 * h; }

template <int N> struct alignas(16) Frags { u16 v[N][64][8]; };
template <int N> struct alignas(16) F4s { float v[N][64][4]; };

constexpr Frags<2> mk_rf1() {
  Frags<2> t{};
  for (int f = 0; f < 2; ++f) for (int l = 0; l < 64; ++l) for (int j = 0; j < 8; ++j) {
    const int d1 = 8 * (l >> 4) + j, k1 = l & 15;
    float v = 0.f;
    if (d1 < 18) v = (f == 0) ? c576(32 * d1 * k1) : -s576(32 * d1 * k1);
    t.v[f][l][j] = tohf(v);
  }
  return t;
}
constexpr F4s<4> mk_rft() {
  F4s<4> t{};
  for (int e = 0; e < 4; ++e) for (int l = 0; l < 64; ++l) for (int q = 0; q < 2; ++q) {
    const int d2 = 16 * (e >> 1) + 4 * (l >> 4) + 2 * (e & 1) + q, k1 = l & 15;
    t.v[e][l][2 * q] = c576(d2 * k1); t.v[e][l][2 * q + 1] = s576(d2 * k1);
  }
  return t;
}
constexpr Frags<8> mk_rf2() {
  Frags<8> t{};
  for (int f = 0; f < 8; ++f) for (int l = 0; l < 64; ++l) for (int j = 0; j < 8; ++j) {
    const int u = f >> 1, d2 = 16 * (f & 1) + 4 * (l >> 4) + (j >> 1), c = j & 1, k2 = 8 * u + ((l & 15) >> 1), co = l & 1;
    const float cs = c576(18 * d2 * k2), sn = s576(18 * d2 * k2);
    float v = co == 0 ? (c == 0 ? cs : sn) : (c == 0 ? -sn : cs);
    if (u >= 2 && co == 1) v = -v;
    t.v[f][l][j] = tohf(v);
  }
  return t;
}
constexpr Frags<4> mk_mid() {
  Frags<4> t{};
  for (int l = 0; l < 64; ++l) for (int j = 0; j < 8; ++j) {
    const int idx = 4 * (l >> 4) + (j >> 1), c = j & 1, o = l & 15;
    const float cs = c2048(128 * idx * o), sn = s2048(128 * idx * o);
    t.v[0][l][j] = tohf(c == 0 ? cs : sn);
    t.v[1][l][j] = tohf(c == 0 ? -sn : cs);
    t.v[2][l][j] = tobf(c == 0 ? cs : -sn);
    t.v[3][l][j] = tobf(c == 0 ? sn : cs);
  }
  return t;
}
constexpr F4s<2> mk_midt() {
  F4s<2> t{};
  for (int e = 0; e < 2; ++e) for (int l = 0; l < 64; ++l) for (int q = 0; q < 2; ++q) {
    const int r = 4 * (l >> 4) + 2 * e + q, c = l & 15;
    t.v[e][l][2 * q] = c2048(8 * r * c); t.v[e][l][2 * q + 1] = s2048(8 * r * c);
  }
  return t;
}
constexpr Frags<4> mk_ri1() {
  Frags<4> t{};
  for (int f = 0; f < 4; ++f) for (int l = 0; l < 64; ++l) for (int j = 0; j < 8; ++j) {
    const int k2 = 8 * (f & 1) + 4 * (l >> 5) + (j >> 1), c = j & 1, d2 = l & 31;
    const float cs = c576(18 * d2 * k2), sn = s576(18 * d2 * k2);
    t.v[f][l][j] = tobf(f < 2 ? (c == 0 ? cs : -sn) : (c == 0 ? sn : cs));
  }
  return t;
}
constexpr F4s<5> mk_rit() {
  F4s<5> t{};
  for (int e = 0; e < 5; ++e) for (int l = 0; l < 64; ++l) for (int q = 0; q < 2; ++q) {
    const int k1 = crow(2 * e + q, l >> 5), d2 = l & 31;
    t.v[e][l][2 * q] = c576(d2 * k1) * (1.f / 9216.f); t.v[e][l][2 * q + 1] = s576(d2 * k1) * (1.f / 9216.f);
  }
  return t;
}
constexpr Frags<3> mk_ri2() {
  Frags<3> t{};
  for (int s = 0; s < 3; ++s) for (int l = 0; l < 64; ++l) for (int j = 0; j < 8; ++j) {
    const int reg = 4 * s + (j >> 1), k1 = crow(reg, l >> 5), c = j & 1, d1 = l & 31;
    float v = 0.f;
    if (reg < 10 && k1 < 18 && d1 < 18) v = (c == 0) ? c576(32 * d1 * k1) : -s576(32 * d1 * k1);
    t.v[s][l][j] = tobf(v);
  }
  return t;
}
struct alignas(1024) Lt { u32 v[24][64]; };
constexpr Lt mk_lt() {
  Lt t{};
  for (u32 l = 0; l < 64; ++l) {
    const u32 g = l >> 4, q = (l & 15) >> 2, p = l & 3;
    const u32 zc = (l & 7) ^ (l >> 3);
    t.v[0][l] = (l >> 2) * 8 * 578 + ((p ^ ((l >> 3) & 3)) * 16) - (l >> 5) * 4;
    t.v[1][l] = (l >> 3) * 13440 + zc * 16;
    t.v[2][l] = (l >> 3) * 128 + zc * 16;
    t.v[3][l] = (l >> 3) * 32896 + (l & 7) * 16;
    (void)g; (void)q;
    for (u32 w = 0; w < 8; ++w) {
      const u32 ca = w * 64 + l, cb = 2048 + (w & 3) * 64 + l;
      t.v[8 + w][l] = (ca / 18) * 1152 + (ca % 18) * 16;
      t.v[16 + w][l] = (cb / 72 + (w >> 2) * 32) * 1152 + (cb % 72) * 4;
    }
  }
  return t;
}
struct alignas(64) Sg { u32 v[16][4]; };
constexpr Sg mk_sg() {
  Sg t{};
  for (int r = 0; r < 4; ++r) for (int o = 0; o < 4; ++o) for (int n = 0; n < 18; ++n) {
    const int c = n < 4 ? r : r ^ ((n - 4) % 3 + 1), rho = n < 4 ? n : (n - 4) / 3;
    const u32 v = n >= 16 ? 9 * ((r >> 1) ^ (n - 16)) + 4 : 9 * (c >> 1) + 5 * (c & 1) + ((rho + o) & 3);
    t.v[4 * o + r][n / 6] |= v << (5 * (n % 6));
  }
  return t;
}
struct alignas(1024) All { Frags<2> rf1; F4s<4> rft; Frags<8> rf2; Frags<2> pad; Frags<4> mid; F4s<2> midt; Frags<4> ri1; F4s<5> rit; Frags<3> ri2; Lt lt; Sg sg; };
constexpr Frags<2> RF1 = mk_rf1(); constexpr F4s<4> RFT = mk_rft(); constexpr Frags<8> RF2 = mk_rf2(); constexpr Frags<4> MID = mk_mid();
constexpr F4s<2> MIDT = mk_midt(); constexpr Frags<4> RI1 = mk_ri1(); constexpr F4s<5> RIT = mk_rit(); constexpr Frags<3> RI2 = mk_ri2();
}
static __device__ const tab::All T_ALL = {tab::RF1, tab::RFT, tab::RF2, {}, tab::MID, tab::MIDT, tab::RI1, tab::RIT, tab::RI2, tab::mk_lt(), tab::mk_sg()};
constexpr int TB_FWD = 0, TB_MID = 16, TB_INV = 22, TB_LT = 34;

constexpr size_t al4k(size_t x) { return (x + 4095) / 4096 * 4096; }
constexpr size_t E_OFF = 0, F2_OFF = 2048, FV_OFF = 4096;
constexpr size_t IB_OFF = 8192;
constexpr size_t RS_OFF = IB_OFF + 256 * 512;
constexpr size_t CS_OFF = RS_OFF + 2048 * 16;
constexpr size_t V_G = 256 * 128 + 128;
constexpr size_t Z_OFF = al4k(CS_OFF + 256 * 2048), Z_BYTES = 4 << 20;
constexpr size_t Z_B = 8 * 1664 + 128;
static_assert(Z_B == 13440 && V_G == 32896, "lane table");
constexpr size_t V_OFF = al4k(Z_OFF + Z_BYTES), V_BYTES = 4 << 20;
constexpr size_t P_OFF = al4k(V_OFF + V_BYTES), P_BLK = 9216, P_BYTES = (size_t)256 * 4 * P_BLK;
constexpr size_t H_OFF = al4k(P_OFF + P_BYTES), H_BYTES = (size_t)16 * 144 * 8 * 1024;

#define ST(n) asm volatile("; STAMP %c0" ::"n"(n))
#define SB() __builtin_amdgcn_sched_barrier(0)
#define WAIT_V(n) asm volatile("s_waitcnt vmcnt(%0)" ::"n"(n) : "memory")
#define WAIT_L0() asm volatile("s_waitcnt lgkmcnt(0)" ::: "memory")
DI void bar() { asm volatile("" ::: "memory"); __builtin_amdgcn_s_barrier(); asm volatile("" ::: "memory"); }
DI void bar_lds() { WAIT_L0(); bar(); }
template <int CTRL> DI float dppf(float v) { return __builtin_bit_cast(float, __builtin_amdgcn_update_dpp(0, __builtin_bit_cast(int, v), CTRL, 0xf, 0xf, true)); }
DI float sum8(float v) { v += dppf<0xB1>(v); v += dppf<0x4E>(v); v += dppf<0x141>(v); return v; }
DI float rdlane(float v, int l) { return __builtin_bit_cast(float, __builtin_amdgcn_readlane(__builtin_bit_cast(int, v), l)); }
DI u32 umin(u32 a, u32 b) { return a < b ? a : b; }
#define TID ((int)__builtin_amdgcn_workitem_id_x())
#define BID ((int)__builtin_amdgcn_workgroup_id_x())
DI float bflo(u32 v) { return __builtin_bit_cast(float, v << 16); }
DI float bfhi(u32 v) { return __builtin_bit_cast(float, v & 0xffff0000u); }
DI u32 pkbf(float a, float b) { bf16x2 v = {(__bf16)a, (__bf16)b}; return __builtin_bit_cast(u32, v); }
DI u32 pkhf(float a, float b) { f16x2 v = {(_Float16)a, (_Float16)b}; return __builtin_bit_cast(u32, v); }
DI float rbf(float a) { return bflo(pkbf(a, a)); }
DI float dot2(u32 a, u32 b, float c) { return __builtin_amdgcn_fdot2_f32_bf16(__builtin_bit_cast(bf16x2, a), __builtin_bit_cast(bf16x2, b), c, false); }
DI f32x4 mfma16b(u32x4 a, u32x4 b, f32x4 c) { return __builtin_amdgcn_mfma_f32_16x16x32_bf16(__builtin_bit_cast(bf16x8, a), __builtin_bit_cast(bf16x8, b), c, 0, 0, 0); }
DI f32x4 mfma16h(u32x4 a, u32x4 b, f32x4 c) { return __builtin_amdgcn_mfma_f32_16x16x32_f16(__builtin_bit_cast(f16x8, a), __builtin_bit_cast(f16x8, b), c, 0, 0, 0); }
DI f32x16 mfma32b(u32x4 a, u32x4 b, f32x16 c) { return __builtin_amdgcn_mfma_f32_32x32x16_bf16(__builtin_bit_cast(bf16x8, a), __builtin_bit_cast(bf16x8, b), c, 0, 0, 0); }
DI void mma(f32x4& c, u32x4 a, u32x4 b) { asm volatile("v_mfma_f32_16x16x32_bf16 %0, %1, %2, %0" : "+v"(c) : "v"(a), "v"(b)); }
DI u32x2 trread(u32 addr) { return __builtin_bit_cast(u32x2, __builtin_amdgcn_ds_read_tr16_b64_v4i16((LAS s16x4*)(size_t)addr)); }
DI float fmul1(float a, float b) { return a * b; }
DI float ffma1(float a, float b, float c) { return __builtin_fmaf(a, b, c); }
DI float fnma1(float a, float b, float c) { return __builtin_fmaf(-a, b, c); }
DI f32x2 cmul(float x, float y, float c, float s) { return f32x2{fnma1(y, s, fmul1(x, c)), ffma1(x, s, fmul1(y, c))}; }
DI f32x2 cmulc(float x, float y, float c, float s) { return f32x2{ffma1(y, s, fmul1(x, c)), fnma1(x, s, fmul1(y, c))}; }
DI u32 pkbf(f32x2 v) { return pkbf(v[0], v[1]); }
DI u32 pkhf(f32x2 v) { return pkhf(v[0], v[1]); }
DI void settle(f32x16& a, f32x16& b) { asm volatile("s_nop 7\n\ts_nop 7" : "+v"(a), "+v"(b)); }
DI void settle(f32x16& a) { asm volatile("s_nop 7\n\ts_nop 7" : "+v"(a)); }
template <typename T> DI T ldsr(u32 addr) { return *(const LAS T*)(size_t)addr; }
template <typename T> DI void ldsw(u32 addr, T v) { *(LAS T*)(size_t)addr = v; }

DI float relu1(float v) { const int b = __builtin_bit_cast(int, v); return __builtin_bit_cast(float, b > 0 ? b : 0); }
DI float sum48(float v) {
  v = sum8(v); v += dppf<0x140>(v);
  return (rdlane(v, 0) + rdlane(v, 16)) + rdlane(v, 32);
}
typedef u32 u32x3 __attribute__((ext_vector_type(3)));
#define LD_(name, T, ins) DI void name(int tg, T& d, const void* sb, u32 vo) { asm volatile(ins " ; TG %c3" : "=v"(d) : "v"(vo), "s"(sb), "n"(tg) : "memory"); }
LD_(gl16, u32x4, "global_load_dwordx4 %0, %1, %2") LD_(gl8, u32x2, "global_load_dwordx2 %0, %1, %2") LD_(gl4, u32, "global_load_dword %0, %1, %2")
LD_(gl16c, u32x4, "global_load_dwordx4 %0, %1, %2 sc1") LD_(gl12c, u32x3, "global_load_dwordx3 %0, %1, %2 sc1") LD_(gl12, u32x3, "global_load_dwordx3 %0, %1, %2") LD_(gl2c, u32, "global_load_ushort %0, %1, %2 sc1") LD_(gl2, u32, "global_load_ushort %0, %1, %2")
#define ST_(name, T, ins, tail) DI void name(int tg, void* sb, u32 vo, T v) { asm volatile(ins " ; TG %c3" tail ::"v"(vo), "v"(v), "s"(sb), "n"(tg) : "memory"); }
ST_(gs16c, u32x4, "global_store_dwordx4 %0, %1, %2 sc1", "\n\ts_nop 1") ST_(gs8c, u32x2, "global_store_dwordx2 %0, %1, %2 sc1", "") ST_(gs8, u32x2, "global_store_dwordx2 %0, %1, %2", "") ST_(gs4c, u32, "global_store_dword %0, %1, %2 sc1", "") ST_(gs12c, u32x3, "global_store_dwordx3 %0, %1, %2 sc1", "\n\ts_nop 1") ST_(gs2, u32, "global_store_short %0, %1, %2", "")
#define DMA_(name, ins) DI void name(int tg, const void* sb, u32 vo, u32 dst, u32 off) { asm volatile("s_add_u32 m0, %2, %4\n\ts_nop 0\n\t" ins " ; TG %c3" ::"v"(vo), "s"(sb), "s"(dst), "n"(tg), "n"(off) : "memory", "scc"); }
DMA_(glds, "global_load_lds_dwordx4 %0, %1") DMA_(glds4, "global_load_lds_dword %0, %1") DMA_(gldsc, "global_load_lds_dwordx4 %0, %1 sc1") DMA_(glds4c, "global_load_lds_dword %0, %1 sc1")
DI u32x4 srd(const void* base, u32 bytes) { const size_t a = (size_t)base; return u32x4{(u32)a, (u32)(a >> 32) & 0xffffu, bytes, 0x00020000u}; }
#define BDMA_(name, ins, pol) DI void name(int tg, u32x4 rs, u32 vo, u32 so, u32 o2, u32 dst, u32 off) { u32 t_; \
  asm volatile("s_add_u32 m0, %4, %6\n\ts_add_u32 %0, %3, %7\n\t" ins " %1, %2, %0 offen" pol " lds ; TG %c5" : "=&s"(t_) : "v"(vo), "s"(rs), "s"(so), "s"(dst), "n"(tg), "n"(off), "n"(o2) : "memory", "scc"); }
BDMA_(blds, "buffer_load_dwordx4", "") BDMA_(blds4, "buffer_load_dword", "") BDMA_(bldsc, "buffer_load_dwordx4", " sc1")
DI void bl16c(int tg, u32x4& d, u32x4 rs, u32 vo, u32 so, u32 o2) { asm volatile("buffer_load_dwordx4 %0, %1, %2, %3 offen offset:%c5 sc1 ; TG %c4" : "=v"(d) : "v"(vo), "s"(rs), "s"(so), "n"(tg), "n"(o2) : "memory"); }
DI void bl16c_rw(int tg, u32x4& d, u32x4 rs, u32 vo, u32 so, u32 o2) { asm volatile("buffer_load_dwordx4 %0, %1, %2, %3 offen offset:%c5 sc1 ; TG %c4" : "+v"(d) : "v"(vo), "s"(rs), "s"(so), "n"(tg), "n"(o2) : "memory"); }
DI void bs16c(int tg, u32x4 rs, u32 vo, u32 so, u32 o2, u32x4 v) { asm volatile("buffer_store_dwordx4 %0, %1, %2, %3 offen offset:%c5 sc1 ; TG %c4\n\ts_nop 1" ::"v"(v), "v"(vo), "s"(rs), "s"(so), "n"(tg), "n"(o2) : "memory"); }
constexpr int WT[64] = {9, 6, 0, 63, 10, 6, 6, 7, 7, 9, 63, 63, 63, 63, 8, 9, 11, 63, 11, 63, 11, 8, 11, 5, 63, 63, 6, 7, 63, 63, 63, 17, 63, 11, 8, 9, 63, 11, 63, 13, 13, 13, 10, 10, 10, 10, 8, 63, 63, 63, 10, 13, 63, 6, 63, 63, 63, 63, 63, 63, 63, 63, 63, 63};
template <int ID, int A, int B = 0, int C = 0, int D = 0> DI void waitfor() { asm volatile("s_waitcnt vmcnt(%0) ; WF %c1 %c2 %c3 %c4 %c5" ::"n"(WT[ID]), "n"(ID), "n"(A), "n"(B), "n"(C), "n"(D) : "memory"); }
template <int I, int N, typename F> DI void sfor(F&& f) { if constexpr (I < N) { f(std::integral_constant<int, I>{}); sfor<I + 1, N>(f); } }
constexpr int STUBS[] = {3081, 1123, 1306, 1456, 2028, 2168, 2319};
constexpr int NSTUB = sizeof(STUBS) / sizeof(int);
constexpr int stub_of(int id) { for (int i = 0; i < NSTUB; ++i) if (STUBS[i] == id) return i; return -1; }
template <int ID> DI void site() {
  if constexpr (stub_of(ID) >= 0) asm volatile("ksite_%c0:\nkstub_%c1:\n\ts_cbranch_execz kback_%c2" ::"n"(ID), "n"(stub_of(ID)), "n"(stub_of(ID) / 8));
  else asm volatile("ksite_%c0:" ::"n"(ID));
}
template <int ID, typename T> DI T sitev(T v) {
  if constexpr (stub_of(ID) >= 0) asm volatile("ksite_%c1:\nkstub_%c2:\n\ts_cbranch_execz kback_%c3" : "+v"(v) : "n"(ID), "n"(stub_of(ID)), "n"(stub_of(ID) / 8));
  else asm volatile("ksite_%c1:" : "+v"(v) : "n"(ID));
  return v;
}
#define SFOR(var, N, ...) sfor<0, N>([&](auto var##_c) LAM { constexpr int var = decltype(var##_c)::value; __VA_ARGS__ })
template <int SET, int K> DI void touch_one(int wid) {
  if constexpr (8 * SET + K < NSTUB) asm volatile("s_cmp_eq_u32 %0, %1\n\ts_cbranch_scc1 kstub_%c2" ::"s"(wid), "n"(K), "n"(8 * SET + K) : "scc", "memory");
}
template <int SET> DI void touch_code(int wid) {
  if constexpr (8 * SET >= NSTUB) return;
  SB();
  asm volatile("s_mov_b64 exec, 0" ::: "memory");
  touch_one<SET, 0>(wid); touch_one<SET, 1>(wid); touch_one<SET, 2>(wid); touch_one<SET, 3>(wid);
  touch_one<SET, 4>(wid); touch_one<SET, 5>(wid); touch_one<SET, 6>(wid); touch_one<SET, 7>(wid);
  asm volatile("kback_%c0:\n\ts_mov_b64 exec, -1" ::"n"(SET) : "memory");
  SB();
}
#define MARK(t) asm volatile("; MK %c0" ::"n"((int)(t)))
#define PATH(s) asm volatile("; PATH " s)
enum { T_ROW = 1, T_TAB, T_MISC, T_RS, T_Z, T_PL1, T_PL2, T_PL3, T_CLR, T_WST, T_FA, T_FB, T_FALL, T_V, T_TI, T_FIN, T_F3, T_IN, T_ZM, T_HS = 30, T_X = 40, T_G = 100, T_W = 200, T_H = 300 };

template <typename T> DI T sfresh(T v) { asm volatile("" : "+s"(v)); return v; }
template <u32 M> DI u32 vand(u32 v) { u32 r; asm volatile("v_and_b32 %0, %2, %1" : "=v"(r) : "v"(v), "n"(M)); return r; }
template <typename T> DI void pin(T& v) { asm volatile("" : "+v"(v)); }
DI bool anylane(bool p) { return __builtin_amdgcn_ballot_w64(p) != 0; }
DI void mmaz(f32x4& d, u32x4 a, u32x4 b) { asm volatile("v_mfma_f32_16x16x32_bf16 %0, %1, %2, 0" : "=&v"(d) : "v"(a), "v"(b)); }
DI void mma16(f32x4& c, u32x2 a, u32x2 b) { asm volatile("v_mfma_f32_16x16x16_bf16 %0, %1, %2, %0" : "+v"(c) : "v"(a), "v"(b)); }
DI void mmaz16(f32x4& d, u32x2 a, u32x2 b) { asm volatile("v_mfma_f32_16x16x16_bf16 %0, %1, %2, 0" : "=&v"(d) : "v"(a), "v"(b)); }
DI f32x4 mfma16k(u32x2 a, u32x2 b, f32x4 c) { return __builtin_amdgcn_mfma_f32_16x16x16bf16_1k(__builtin_bit_cast(s16x4, a), __builtin_bit_cast(s16x4, b), c, 0, 0, 0); }
constexpr int SPIN_CAP = 1 << 15;
#define POLL(BAD, RELOAD, PINS) do { if (__builtin_expect(anylane(BAD), 0)) { int sp_ = 0; \
    do { __builtin_amdgcn_s_sleep(2); RELOAD; asm volatile("s_waitcnt vmcnt(0) ; POLLW" ::: "memory"); PINS; } while (anylane(BAD) && ++sp_ < SPIN_CAP); } } while (0)

constexpr u32 SLOT = 24576, SLOT2 = 18432;
constexpr u32 L_TAB = SLOT, L_ROWS = SLOT + 14336;
constexpr u32 L_STG = 6 * SLOT, L_CB = L_STG, L_CZ = L_CB + 3072, L_LT = L_STG + 4096, L_CA = L_STG + 10240, L_RI = L_CA + 320 * 16, L_DUMP = L_RI + 32, L_END = L_DUMP + 256;
constexpr u32 L_W = 0, L_TM = 32768, L_Z = 40960;
constexpr u32 L_Y = L_STG, YP = 19 * 64, L_S = 4 * SLOT2, L_TI = 6 * SLOT2, L_XC = 0, L_V = 7 * SLOT2;
static_assert(L_END <= 163840, "LDS");

__global__ NOPK __launch_bounds__(512, 2) __attribute__((aligned(4096))) void k_all(const u16* __restrict__ x, const u16* __restrict__ wr, const u16* __restrict__ wi,
                                                     const u16* __restrict__ w1, const u16* __restrict__ b1, const u16* __restrict__ w2,
                                                     const u16* __restrict__ b2, char* __restrict__ ws, u16* __restrict__ out) {
  constexpr u32 lds = 0;
  asm volatile("" ::"s"(x), "s"(wr), "s"(wi), "s"(w1), "s"(b1), "s"(w2), "s"(b2), "s"(ws), "s"(out));
  const int wid = __builtin_amdgcn_readfirstlane(TID >> 6);
  const int xcd = BID & 7, idx = BID >> 3, b = 32 * xcd + idx, r_ = idx & 3, i_ = (idx >> 2) + 8 * (xcd >> 2), q_ = xcd & 3, j_ = 4 * q_ + r_;
  const int mg = wid & 3, ng = wid >> 2, odd = r_ & 1, rot = i_ & 3, kap = (r_ + (idx >> 4)) % 3;
  const char* tb = (const char*)&T_ALL;
  const u32 zrot = ((b >> 3) & 1) + ((b >> 5) & 2);
  u32 hvp = (TID & 63) * 16;
#define LANES u32 lane = hvp; pin(lane); lane >>= 4; const u32 tid = wid * 64 + lane, g = lane >> 4, q = (lane & 15) >> 2, p = lane & 3; (void)tid; (void)g; (void)q; (void)p
#define TRROW (lds + (8 * g + q) * 128 + p * 8)
#define FSW ((q >> 1) | ((g & 1) << 1))
  ST(0);
  const int c0 = 288 * j_ - 32 * odd;
  const char* w1c = (const char*)w1 + c0 * 2;
  auto slab = [&](int r) LAM { return r == 4 ? (odd ? 0 : 4) : (((r + rot) & 3) + odd); };
  auto kgrp = [&](int gi) LAM { const int t = gi + kap; return t >= 3 ? t - 3 : t; };
  u32 voffw, vx0, vx1;
  const u32 dstw = lds + wid * 1024, dump = sfresh(L_DUMP);
  const u32x4 rw1 = srd(w1, D_ * F_ * 2);
  u32 sw1 = 0;
  auto issue_w1 = [&](int n, int u) LAM {
    if (u == 0) sw1 = sfresh((u32)(c0 * 2 + slab(n / 3) * 128 + kgrp(n % 3) * 192 * 9216));
    blds(T_G + n, rw1, voffw, sw1, u * 64 * 9216, dstw, (n % 6) * SLOT + u * 8192);
  };
  const u32x4 rx = srd(x, S_ * D_ * 2);
  u32 sx = 0;
  auto issue_x = [&](int gi, int e) LAM {
    if (e == 0) sx = sfresh((u32)((2 * (wid & 1) + 32 * (wid >> 1) + 128 * (i_ >> 3) + 4 * (i_ & 7)) * 1152 + kgrp(gi) * 384));
    blds(T_X + gi, rx, (e & 1) ? vx1 : vx0, sx, (e >> 1) * 128, 2 * dstw, (gi == 1 ? 2 : 4) * SLOT + (e >> 1) * 16384 + (e & 1) * 1024);
  };
  u32 T0, T1, G0, G1;
  unsigned long long TG, TU;
  auto bad_unit = [&](u32x4 v) LAM { typedef unsigned long long u64_; return ((((u64_)v[1] << 32 | v[0]) >> 16) != TU) | ((((u64_)v[3] << 32 | v[2]) >> 16) != TU); };
  auto bad_flags = [&](u32x4 v) LAM { typedef unsigned long long u64_; return (((u64_)v[1] << 32 | v[0]) != TG) | (((u64_)v[3] << 32 | v[2]) != TG); };
  u32 tra[3], trb[3], cab, cbb, csa, xa[2][2], sa0, sa1, zo0, cbw;
  u32x4 r8, r8i;
  u32x4 st[2][18];
  auto xmove = [&](u32 d) LAM { xa[0][0] += d; xa[0][1] += d; xa[1][0] += d; xa[1][1] += d; pin(xa[0][0]); pin(xa[0][1]); pin(xa[1][0]); pin(xa[1][1]); };
#define RDX(ks, m) st[m][ks] = ldsr<u32x4>(xa[(ks) & 1][m] + (((ks) % 6) >> 1) * 16384)
  u32 sg0, sg1, sg2, dstb;
  u32x4 rw2;
  const char *ibx, *wsrc, *xp, *b2p;
  char* outp;
  auto late_scalars = [&]() LAM {
    const int bb_ = sfresh(b), wd = sfresh(wid), xc = bb_ >> 5, ix = bb_ & 31;
    sg0 = T_ALL.sg.v[ix & 15][0]; sg1 = T_ALL.sg.v[ix & 15][1]; sg2 = T_ALL.sg.v[ix & 15][2];
    rw2 = srd((const char*)w2 + (size_t)(xc & 3) * 1152 * 1152 + (ix & 3) * 288, 1152 * 1152 - (ix & 3) * 288);
    dstb = sfresh(lds + (wd >> 2) * 9216 + (wd & 3) * 256);
    ibx = sfresh(ws + IB_OFF + bb_ * 512 + (xc & 3) * 32);
    xp = sfresh((const char*)x + (size_t)bb_ * 1152); b2p = sfresh((const char*)b2); outp = sfresh((char*)out + (size_t)bb_ * 1152);
    wsrc = sfresh((const char*)(wd >> 2 ? wi : wr));
  };
  u32 lta;
  {
    LANES;
    u32x3 xv[2];
    const char* xr = (const char*)x + (size_t)(256 * wid + b) * 1152;
    gl12(T_ROW, xv[0], xr, umin(lane, 47u) * 12); gl12(T_ROW, xv[1], xr + 576, umin(lane, 47u) * 12);
    u32x4 ep; gl16c(T_ROW, ep, ws + E_OFF, vand<0>(lane));
    ST(29);
    glds(T_TAB, tb + umin(wid, 6) * 2048, hvp, lds + umin(wid, 6) * 2048, L_TAB);
    glds(T_TAB, tb + umin(wid, 6) * 2048 + 1024, hvp, lds + umin(wid, 6) * 2048, L_TAB + 1024);
    const u32 lr = lane >> 3, lc = lane & 7;
    vx0 = 256 * lr * 1152 + ((lc ^ (lr >> 1)) * 16); vx1 = (vx0 ^ 64) + 1152;
    issue_x(0, 0); issue_x(0, 1);
    u32 bb;
    {
      const float sc = (g >> 1) == ((lane >> 3) & 1) ? 1.f / 64.f : 0.f;
      SFOR(e, 4,
        const float an = (float)(((256 * (4 * (g & 1) + e) + b) * lc) & 2047) * (1.f / 2048.f);
        const float c = __builtin_amdgcn_cosf(an) * sc; const float s = __builtin_amdgcn_sinf(an) * sc;
        r8[e] = pkhf(c, s); r8i[e] = pkhf(-s, c);
        if constexpr (e & 1) { pin(r8); pin(r8i); issue_x(0, 2 + (e >> 1)); }
      );
      xa[0][0] = lds + 4 * SLOT + (2 * mg + ng) * 2048 + (lane & 15) * 128 + ((g ^ ((lane & 15) >> 1)) * 16);
      xa[1][0] = xa[0][0] ^ 64; xa[0][1] = xa[0][0] ^ 2048; xa[1][1] = xa[0][0] ^ 2112;
      voffw = (lr + wid * 8) * 9216u + ((((lc >> 1) ^ (((lr >> 1) & 1) | ((wid & 1) << 1))) << 1) | (lc & 1)) * 16;
      pin(xa[0][0]); pin(xa[0][1]); pin(xa[1][0]); pin(xa[1][1]); pin(voffw);
      issue_x(0, 4);
      tra[0] = TRROW + ((2 * ng) ^ FSW) * 32;
#pragma unroll
      for (int e = 0; e < 3; ++e) { tra[e] = tra[0] + 2 * e * SLOT; trb[e] = tra[e] ^ 32; pin(tra[e]); pin(trb[e]); }
      csa = (tra[0] ^ ((mg & 1) * 32)) + (mg >> 1) * 3 * 4096;
      const u32 fq = 24 * wid + 12 * (g >> 1) + 3 * q + umin((p - zrot) & 3, 2u);
      sa0 = lds + L_STG + fq * 16 + (g & 1) * 5120;
      sa1 = lds + L_STG + umin(fq + 192, 288u) * 16 + (g & 1) * 5120;
      pin(sa0); pin(sa1); pin(csa);
      issue_x(0, 5);
      zo0 = lc * 1664 + ((lane >> 3) & 1) * 64 + g * 16;
      pin(zo0);
      issue_w1(0, 0);
      cab = lds + L_CA + (ng * 32 + (lane & 15)) * 16 + (g & 1) * 8; cbb = lds + (g < 2 ? L_CB + mg * 768 + ng * 256 + (lane & 15) * 16 + g * 8 : L_CZ);
      cbw = lds + L_CB + (wid >> 1) * 768 + ((wid & 1) ? 256 + (hvp & 255) : (hvp & 511) + (hvp & 256));
      lta = lds + L_LT + lane * 4;
      pin(cab); pin(cbb); pin(cbw); pin(lta);
    }
    if (wid < 3) gl4(0, bb, b1 + c0, umin(tid, 159) * 4);
    if (wid == 3) glds4(0, tb + TB_MID * 1024 + idx * 768, umin(lane, 5u) * 128, dump, 0);
    ST(30);
    touch_code<0>(wid);
    ST(19);
    waitfor<0, T_ROW>();
    pin(xv[0]); pin(xv[1]); pin(ep);
    const unsigned long long tg = (((unsigned long long)(u32)__builtin_amdgcn_readfirstlane(ep[1]) << 32) | (u32)__builtin_amdgcn_readfirstlane(ep[0])) + 0x9E3779B97F4A7C15ull;
    ST(1);
    T0 = (u32)tg; T1 = (u32)(tg >> 32); G0 = T0 << 16; G1 = (u32)(tg >> 16); TG = tg; TU = tg & 0xffffffffffffull;
    float sm = 0.f, sq = 0.f, sm2 = 0.f, sq2 = 0.f;
#pragma unroll
    for (int u = 0; u < 3; ++u) { sm = dot2(xv[0][u], 0x3f803f80u, sm); sq = dot2(xv[0][u], xv[0][u], sq); sm2 = dot2(xv[1][u], 0x3f803f80u, sm2); sq2 = dot2(xv[1][u], xv[1][u], sq2); }
    sm += sm2; sq += sq2; pin(sm); pin(sq);
    issue_w1(0, 1);
    sm = sum48(sm); sq = sum48(sq); pin(sm); pin(sq);
    issue_w1(0, 2);
    const float mean = sm * (1.f / 576.f);
    const float var = __builtin_fmaxf(sq * (1.f / 576.f) - mean * mean, 0.f);
    const float mb = rbf(mean), den = rbf(__builtin_amdgcn_sqrtf(rbf(rbf(var) + 1e-5f)));
    const float inv = __builtin_amdgcn_rcpf(den);
    gs16c(T_RS, ws + RS_OFF + ((i_ * 16 + j_) * 8 + wid) * 16, vand<0>(lane),
          u32x4{G0 | (__builtin_bit_cast(u32, -mb) >> 16), G1, G0 | (__builtin_bit_cast(u32, den) >> 16), G1});
    ldsw<float>(lds + L_RI + wid * 4, inv * inv);
    const float mi = -mb * inv;
    const u32 rw = lds + L_ROWS + wid * 1280 + umin(lane, 47u) * 12;
    SFOR(u, 6,
      const u32 v = xv[u / 3][u % 3];
      ldsw<u32>(rw + (u / 3) * 576 + (u % 3) * 4, pkhf(__builtin_fmaf(bflo(v), inv, mi), __builtin_fmaf(bfhi(v), inv, mi))); site<3010 + u>();
      if constexpr (u % 3 == 2) issue_x(1, u / 3);
    );
    ST(20);
    waitfor<1, T_TAB, T_X + 0>();
    bar();
    ST(2);
    const u32 rb = lds + L_ROWS + wid * 1280 + p * 8, r0 = rb + umin(8 * g + q, 17u) * 64, r1 = rb + umin(8 * g + 4 + q, 17u) * 64;
    const u32 ta = lds + L_TAB + hvp;
    const f32x4 z = {0.f, 0.f, 0.f, 0.f};
    u32x4 tf[14];
    u32x2 lo[2], hi[2];
#pragma unroll
    for (int t = 0; t < 2; ++t) { lo[t] = trread(r0 + t * 32); hi[t] = trread(r1 + t * 32); }
    SFOR(e, 14, tf[e] = ldsr<u32x4>(ta + e * 1024); if constexpr (e % 4 == 1) site<3020 + e>(););
    f32x4 tr[2], ti[2];
#pragma unroll
    for (int t = 0; t < 2; ++t) { const u32x4 a = {lo[t][0], lo[t][1], hi[t][0], hi[t][1]}; tr[t] = mfma16h(a, tf[0], z); ti[t] = mfma16h(a, tf[1], z); }
    SB();
    issue_x(1, 2);
    SFOR(k_, 6, RDX(k_, 0); RDX(k_, 1); SB(); site<3075 + k_>(););
    issue_x(1, 3);
    asm volatile("" : "+v"(tr[0]), "+v"(ti[0]), "+v"(tr[1]), "+v"(ti[1]));
    u32x4 pk[2];
    SFOR(e, 4,
      const f32x4 tw = __builtin_bit_cast(f32x4, tf[2 + e]);
      constexpr int t = e >> 1; constexpr int o = 2 * (e & 1);
      pk[t][o] = pkhf(cmulc(tr[t][o], ti[t][o], tw[0], tw[1]));
      pk[t][o + 1] = sitev<3041 + 2 * e>(pkhf(cmulc(tr[t][o + 1], ti[t][o + 1], tw[2], tw[3])));
      if constexpr (e & 1) issue_x(1, 4 + (e >> 1));
    );
    f32x4 X[4];
#pragma unroll
    for (int u = 0; u < 4; ++u) X[u] = mfma16h(tf[6 + 2 * u], pk[0], z);
#pragma unroll
    for (int u = 0; u < 4; ++u) X[u] = mfma16h(tf[7 + 2 * u], pk[1], X[u]);
    SB();
    xmove(-2 * SLOT);
    site<3050>();
    asm volatile("s_nop 5" : "+v"(X[0]), "+v"(X[1]), "+v"(X[2]), "+v"(X[3]));
    const u32 k1 = lane & 15;
    if (k1 < 10) {
      const u32 b1_ = lds + L_STG + ng * 5120 + mg * 4 + (k1 + 36 * g) * 16, b2_ = lds + L_STG + ng * 5120 + mg * 4 + (117 - k1 - 36 * g) * 16;
#pragma unroll
      for (int u = 0; u < 4; ++u)
#pragma unroll
        for (int h = 0; h < 2; ++h) ldsw<u32>(u < 2 ? b1_ + (144 * u + 18 * h) * 16 : b2_ + (459 - 144 * u - 18 * h) * 16, pkhf(X[u][2 * h], X[u][2 * h + 1]));
    }
    site<3053>();
    touch_code<1>(wid);
    site<3056>();
    late_scalars();
    ST(21);
    waitfor<2, T_X + 1, T_G + 0>();
    pin(bb);
    if (tid < 160) { ldsw<u32x2>(lds + L_CA + tid * 32 + 8, u32x2{bb & 0xffffu, 0u}); ldsw<u32x2>(lds + L_CA + tid * 32 + 24, u32x2{bb >> 16, 0u}); }
    bar_lds();
    ST(3);
    site<3057>();
  }

  const u32x4 rh = srd(ws + H_OFF + ((size_t)(i_ * 144 + q_ * 36 + ng) * 8 + 2 * mg) * 1024, 36 * 8192);
  const int hs0 = (9 * r_ - odd) / 2;
  u32x2 wf[4][4];
  f32x4 acc[2][4];
#define RD1(slot, kk, sl, dst) wf[dst][sl] = trread((((sl) >> 1) ? trb[(slot) >> 1] : tra[(slot) >> 1]) + ((slot) & 1) * SLOT + (kk) * 4096 + ((sl) & 1) * 512)
#define FRAG(src, t) u32x4{wf[src][2 * (t)][0], wf[src][2 * (t)][1], wf[src][2 * (t) + 1][0], wf[src][2 * (t) + 1][1]}
  const u32x4 ones = {0x3f803f80u, 0x3f803f80u, 0x3f803f80u, 0x3f803f80u};
  auto put_colsum = [&](f32x4 cs, int sl, int half, bool publish) LAM {
    const float v = cs[0], hi = rbf(v);
    const u32 w = pkbf(hi, v - hi), co = vand<255>(hvp), cu = half * 512 + (mg & 1) * 256;
    ldsw<u32>(lds + L_CA + sl * 1024 + cu + (mg >> 1) * 4 + co, w);
    if (publish) gs16c(T_RS, ws + CS_OFF + (b * 2 + (mg >> 1)) * 1024 + cu, co, u32x4{G0 | (w & 0xffffu), G1, G0 | (w >> 16), G1});
  };
  f32x4 cs;
  u32x4 pl;
  {
    u32x4 zs[2];
    const u32x4 a0 = ldsr<u32x4>(sa0), a1 = ldsr<u32x4>(sa1);
    SFOR(e, 11, RD1(0, e >> 2, e & 3, e >> 2); if constexpr (e % 4 == 0) { SB(); site<3060 + e>(); });
    SB();
    {
      const f32x4 z = {0.f, 0.f, 0.f, 0.f};
      f32x4 re0 = mfma16h(a0, r8, z), im0 = mfma16h(a0, r8i, z), re1 = mfma16h(a1, r8, z), im1 = mfma16h(a1, r8i, z);
      SB();
      issue_w1(1, 0); site<3081>(); issue_w1(1, 1); site<3082>(); issue_w1(1, 2); site<3083>();
      SFOR(k_, 6, RDX(k_ + 6, 0); RDX(k_ + 6, 1); SB(); site<3090 + k_>(););
      asm volatile("" : "+v"(re0), "+v"(im0), "+v"(re1), "+v"(im1));
      SFOR(e, 4, zs[0][e] = pkhf(re0[e], im0[e]); zs[1][e] = pkhf(re1[e], im1[e]););
      zs[0] = sitev<3084>(zs[0]); zs[1] = sitev<3085>(zs[1]);
    }
    u32x4 onesv = ones;
    pin(onesv);
    xmove(2 * SLOT);
    ST(4);
    u32x2 cf[2];
    sfor<0, 18>([&](auto ksc) LAM {
      constexpr int ks = decltype(ksc)::value, kr = ks + 3;
      if constexpr (ks == 1 || ks == 3 || ks == 9 || ks == 13 || ks == 15) {
        SB();
        if constexpr (ks == 1) asm volatile("s_waitcnt lgkmcnt(4)" ::: "memory");
        if constexpr (ks == 3) waitfor<4, T_G + 1>();
        if constexpr (ks == 9) waitfor<5, T_G + 2, T_X + 2>();
        if constexpr (ks == 13) asm volatile("s_waitcnt lgkmcnt(4)" ::: "memory");
        if constexpr (ks == 15) waitfor<6, T_G + 3, T_Z>();
        bar();
        ST(ks == 1 ? 14 : ks == 3 ? 15 : ks == 9 ? 16 : ks == 13 ? 17 : 18);
        if constexpr (ks == 15) { if (wid == 0 && hvp == 0) gs8c(0, ws + F2_OFF + b * 8, hvp, u32x2{sfresh(T0), sfresh(T1)}); }
      }
      sfor<0, 4>([&](auto slc) LAM {
        constexpr int sl = decltype(slc)::value, t = sl >> 1, m = sl & 1, n = ks * 4 + sl;
        SB();
        site<1000 + n>();
        if (ks == 0) mmaz(acc[0][sl], FRAG(ks % 4, t), st[m][ks]); else mma(acc[0][sl], FRAG(ks % 4, t), st[m][ks]);
        SB();
        if constexpr (n >= 36 && n < 48) RDX(12 + ((n - 36) >> 1), n & 1);
        { const int kq = sl == 0 ? kr - 1 : kr, sq = (sl + 3) & 3; RD1(kq / 6, kq % 6, sq, kq % 4); }
        SB();
        if constexpr ((ks & 1) == 0 && sl < 2) cf[sl] = trread(csa + (ks / 6) * SLOT + ((ks % 6) >> 1) * 4096 + sl * 512);
        if ((ks & 1) == 1 && sl == 3) { const u32x4 a = {cf[0][0], cf[0][1], cf[1][0], cf[1][1]}; if (ks == 1) mmaz(cs, onesv, a); else mma(cs, onesv, a); }
        if (n < 12 && (n & 1) == 0) issue_x(2, n >> 1);
        if (n >= 5 && n < 11 && (n & 1)) issue_w1(2, (n - 5) >> 1);
        if (n >= 14 && n < 26 && (n & 3) == 2) issue_w1(3, (n - 14) >> 2);
        if constexpr (n == 16) gs16c(T_Z, ws + Z_OFF + (size_t)b * Z_B + wid * 128, zo0, zs[0]);
        if constexpr (n == 12) { if (wid < 5) gs16c(0, ws + Z_OFF + (size_t)b * Z_B + (8 + wid) * 128, zo0, zs[1]); }
        if (n == 40) { LANES; glds4(T_MISC, (const char*)(ng ? wi : wr) + (size_t)(xcd + 64 * idx + 16 * mg) * 578, ((lane >> 3) & 1) * 8 * 578 + umin((lane & 7) * 128, 574u), dump, 0); }
        if (n == 44) { LANES; glds4(T_MISC, w1c + slab(4) * 128 + (size_t)(((idx >> 2) * 2 + odd) * 36 + wid * 4) * 9216, umin(lane, wid == 7 ? 7u : 3u) * 9216, dump, 0); }
        if constexpr (n == 50) {
          waitfor<7, T_PL1>();
          pin(pl);
          POLL(bad_unit(pl), gl16c(0, pl, ws + RS_OFF + (i_ * 8 + wid) * 256, vand<255>(hvp)), pin(pl));
        }
        if constexpr (n == 54) {
          const u32 mm = __builtin_amdgcn_perm(pl[0], pl[0], 0x01000100u);
          ldsw<u32x4>(cbw, u32x4{mm, mm, pl[2] & 0xffffu, 0u});
        }
        if constexpr (n == 58) ldsw<u32x2>(lds + L_CZ + vand<16>(hvp) * 16, u32x2{0u, 0u});
        if (n == 11) gl16c(T_PL1, pl, ws + RS_OFF + (i_ * 8 + wid) * 256, vand<255>(hvp));
        if (n >= 53 && n < 71 && (n - 53) % 3 == 0) issue_w1(4 + (n - 53) / 9, ((n - 53) % 9) / 3);
        if (n == 30) { const u32 o = umin(wid, 5) * 1024; glds(T_MISC, tb + TB_LT * 1024 + o, hvp, lds + o, L_LT); }
      });
    });
#undef RDX
  }
  ST(5);
  site<3382>();
  SB();
  asm volatile("s_nop 7\n\ts_nop 1" : "+v"(cs));
  put_colsum(cs, slab(0), ng, true);
  SB();
  u32 sw2[2] = {0, 0}, shq = 0;
  auto sig = [&](int n) LAM {
    u32 v; asm volatile("s_bfe_u32 %0, %1, %2" : "=s"(v) : "s"(n < 6 ? sg0 : n < 12 ? sg1 : sg2), "n"(0x50000 + 5 * (n % 6)) : "scc"); return v;
  };
  u32 voffa, voffb;
  auto issue_w2 = [&](int n, int u) LAM {
    const u32 d = ((n + 4) & 7) * SLOT2;
    if (u == 0) { sw2[n & 1] = sig(n) * (64u * 1152u); blds(T_W + n, rw2, voffa, sw2[n & 1], 0, dstw, d); }
    if (u == 1) blds(T_W + n, rw2, voffa, sw2[n & 1], 32 * 1152, dstw, d + 9216);
    if (u == 2) blds4(T_W + n, rw2, voffb, sw2[n & 1], 0, dstb, d + 8192);
  };
  u32x4 hf[10][2];
  auto issue_h = [&](int n, int m) LAM { if (m == 0) shq = sig(n) * 16384u; bl16c(T_H + n, hf[n % 10][m], rh, hvp, shq, m * 1024); };
  auto issue_hp = [&](auto kc) LAM {
    constexpr int k = decltype(kc)::value, n = 4 + k / 2, m = k & 1;
    if (m == 0) shq = sig(n) * 16384u;
    bl16c_rw(T_H + n, hf[n][m], rh, hvp, shq, m * 1024);
  };
  auto post = [&](int stage) LAM {
    if (wid == 0 && hvp < (stage == 3 ? 256 : 64))
      gs8c(0, sfresh(ws) + IB_OFF + (stage == 3 ? (b & ~99) : (b & ~3)) * 512 + stage * 128 + j_ * 8, vand<48>(hvp) * 32 + vand<192>(hvp) * 256, u32x2{sfresh(T0), sfresh(T1)});
  };
  auto peek = [&](int tg, u32x4& d, int stage) LAM { gl16c(tg, d, sfresh(ibx) + (stage == 3 ? 384 - q_ * 32 : stage * 128), stage == 3 ? vand<112>(hvp) : vand<16>(hvp)); };
  u32x4 fl;
  int idle = odd;
  u32x4 rd;
  u32 vtmp;
  const u32 tsk = (odd && idx < 26) ? ~0u : 0u;
  auto lt = [&](int k) LAM { return ldsr<u32>(lta + k * 256); };
  auto round = [&](auto rc) LAM {
    constexpr int r = decltype(rc)::value, P = r & 1, C0 = r >= 3 ? 0 : 4;
    const u32 hso = (hs0 + slab(r - 1)) * 16384;
    const u32 cao = slab(r - 1) * 1024;
    float y0, y1;
    u32 pk[8];
    u32x2 ca[2], cb[2], cf4[2][2];
    u32x4 on4;
    sfor<0, 18>([&](auto ksc) LAM {
      constexpr int ks = decltype(ksc)::value, kr = ks + 3, c = 18 * r + ks, nb = 3 * r + kr / 6;
      if constexpr (kr % 6 == 0 && nb <= 12) {
        SB();
        if constexpr (nb >= 8) ST(26 + nb);
        if constexpr (nb == 0) {
        } else if constexpr (nb == 12) waitfor<10 + nb, T_G + 12>();
        else if constexpr (nb == 11) waitfor<10 + nb, T_G + nb, T_HS + r - 2>();
        else waitfor<10 + nb, T_G + nb>();
        if (nb == 4 || nb == 7) asm volatile("s_waitcnt lgkmcnt(4)" ::: "memory");
        bar();
        ST(51 + nb);
        if (nb == 11) post(0);
      }
      sfor<0, 4>([&](auto slc) LAM {
        constexpr int sl = decltype(slc)::value, t = sl >> 1, m = sl & 1, n = ks * 4 + sl, ne = n - 4 * C0;
        SB();
        site<1000 + 100 * r + n>();
        if (ks == 0) mmaz(acc[P][sl], FRAG(c % 4, t), st[m][ks]); else mma(acc[P][sl], FRAG(c % 4, t), st[m][ks]);
        SB();
        { const int kq = sl == 0 ? kr - 1 : kr, sq = (sl + 3) & 3;
          if (18 * r + kq < 90) RD1((3 * r + kq / 6) % 6, kq % 6, sq, (18 * r + kq) % 4); }
        SB();
        if (ne == 0) ca[0] = ldsr<u32x2>(cab + cao);
        if (ne == 1) cb[0] = ldsr<u32x2>(cbb);
        if (ne == 2) cb[1] = ldsr<u32x2>(cbb + 256);
        if (ne == 3) ca[1] = ldsr<u32x2>(cab + cao + 256);
        if constexpr (ne >= 8 && ne < 12) mma16(acc[1 - P][ne - 8], ca[(ne - 8) >> 1], cb[(ne - 8) & 1]);
        if constexpr (ne >= 14 && ne < 46) {
          constexpr int e = ne - 14, pr = e >> 2, tl = (pr >> 2) + (pr & 2), hh = pr & 1;
          if ((e & 3) == 0) asm volatile("v_mul_f32 %0, %1, abs(%1)" : "=v"(y0) : "v"(acc[1 - P][tl][2 * hh]));
          if ((e & 3) == 1) asm volatile("v_mul_f32 %0, %1, abs(%1)" : "=v"(y1) : "v"(acc[1 - P][tl][2 * hh + 1]));
          if ((e & 3) == 2) asm volatile("v_cvt_pk_bf16_f32 %0, %1, %2" : "=v"(pk[pr]) : "v"(y0), "v"(y1));
          if ((e & 3) == 3) asm volatile("v_pk_max_i16 %0, %0, 0" : "+v"(pk[pr]));
        }
        if constexpr (ne == 30 || ne == 46) { constexpr int mm = (ne - 30) / 16; hf[r - 1][mm] = u32x4{pk[4 * mm], pk[4 * mm + 1], pk[4 * mm + 2], pk[4 * mm + 3]}; bs16c(T_HS + r - 1, rh, hvp, hso, mm * 1024, hf[r - 1][mm]); }
        if constexpr ((r < 3 && n >= 15 && (n - 15) % 7 == 0) || (r == 3 && n < 30 && (n == 3 || (n >= 8 && (n - 8) % 7 == 0)))) {
          constexpr int pi = r < 3 ? 9 * r + 13 + (n - 15) / 7 : (n == 3 ? 40 : 41 + (n - 8) / 7), nq = pi / 3, u = pi - 36;
          if constexpr (nq <= 11) issue_w1(nq, pi % 3);
          else {
            if constexpr (u == 0) rd = odd ? srd(wsrc, S_ * NKF * 2 & tsk) : rw1;
            if constexpr (u == 4) rd = odd ? srd(tb + TB_MID * 1024, 6144 & tsk) : rw1;
            if constexpr (u == 5) rd = odd ? srd(ws + Z_OFF, (u32)Z_BYTES & tsk) : rw1;
            const u32 se = c0 * 2 + 512 + (kgrp(nq - 12) * 192 + (u % 3) * 64) * 9216;
            const u32 so = u < 4 ? ((xcd + 512 * mg + 128 * u) * NKF + 12 * (idx - 1) - (xcd & 1) - 4 * (mg & 1)) * 2 : u == 4 ? dstw : ((u - 5) * 64 + wid * 8) * (u32)Z_B + xcd * 1664 + (idx - 1) * 64;
            if constexpr (u < 5) blds(T_G + nq, rd, voffw, sfresh(odd) ? so : se, 0, dstw, u * 8192); else bldsc(T_G + nq, rd, voffw, sfresh(odd) ? so : se, 0, dstw, u * 8192);
          }
        }
        if ((r == 2 && n == 44) || (r == 3 && n == 1)) vtmp = lt(r - 2);
        if (r == 2 && n == 48) { if (odd) voffw = vtmp; pin(voffw); }
        if (r == 3 && n == 2) { if (odd) voffw = hvp; pin(voffw); }
        if (r == 3 && n == 6) { if (odd) voffw = vtmp; pin(voffw); }
        if (r < 3 && n == 11) blds4(T_MISC, rw2, umin(vand<1023>(hvp) * 8, 2560u), (idx * 8 + wid) * 5184, (r - 1) * 2560, dump, 0);
        if (r == 1) {
          if (n == 2 || n == 5 || n == 8) issue_w1(6, n / 3);
          if (n == 13) issue_w1(7, 0);
          if (n == 38) gl16c(T_PL2, pl, ws + CS_OFF + ((b & ~12) | (((((int)umin(wid >> 1, 2) + 1 + rot) & 3)) << 2)) * 2048 + (wid & 1) * 1024, hvp);
        }
        if constexpr (r == 2 && n == 5) {
          waitfor<8, T_PL2>();
          pin(pl);
          POLL(bad_unit(pl), gl16c(0, pl, ws + CS_OFF + ((b & ~12) | (((((int)umin(wid >> 1, 2) + 1 + rot) & 3)) << 2)) * 2048 + (wid & 1) * 1024, hvp), pin(pl));
        }
        if constexpr (r == 2 && n == 7) ldsw<u32>(lds + L_CA + (((((int)umin(wid >> 1, 2) + 1 + rot) & 3) + odd) * 1024 + (wid & 1) * 4) + hvp, __builtin_amdgcn_perm(pl[2], pl[0], 0x05040100u));
        if constexpr (r == 2 && n == 59) {
          waitfor<9, T_PL3>();
          pin(pl);
          POLL(bad_flags(pl), gl16c(0, pl, ws + F2_OFF + wid * 256, vand<255>(hvp)), pin(pl));
        }
        if (r == 2 && n == 9) gl16c(T_PL3, pl, ws + F2_OFF + wid * 256, vand<255>(hvp));
        if (r == 3) {
          if (n == 34) { voffa = ldsr<u32>(lta + dstw / 4 + 8 * 256); voffb = ldsr<u32>(lta + dstw / 4 + 16 * 256); }
          if (n == 39 || n == 49 || n == 55) issue_w2(0, (n - 39) >> 3);
          if (n == 62 || n == 66 || n == 70) issue_w2(1, (n - 62) >> 2);
        }
        if constexpr (r == 4) {
          if constexpr (n == 12) ST(49);
          if constexpr (n == 24) ST(50);
          if constexpr (n == 27) ST(51);
          if constexpr (n == 48) ST(52);
          if constexpr (n == 24) { csa = (tra[0] ^ ((mg & 1) * 32)) + (mg >> 1) * 3 * 4096; pin(csa); on4 = ones; pin(on4); }
          if constexpr (n >= 28 && n < 64) {
            constexpr int e = (n - 28) >> 2;
            if constexpr (sl < 2) cf4[e & 1][sl] = trread(csa + (e / 3) * SLOT + (e % 3) * 4096 + sl * 512);
          }
          if constexpr (n >= 35 && n < 71 && sl == 3) {
            constexpr int e = (n - 35) >> 2;
            const u32x4 a = {cf4[e & 1][0][0], cf4[e & 1][0][1], cf4[e & 1][1][0], cf4[e & 1][1][1]};
            if (e == 0) mmaz(cs, on4, a); else mma(cs, on4, a);
          }
          if constexpr (n == 33) { waitfor<27, T_F3>(); pin(fl); POLL(bad_flags(fl), peek(0, fl, 0), pin(fl)); }
          if constexpr (n >= 36 && (n - 36) % 3 == 0) issue_h(4 + (n - 36) / 6, ((n - 36) / 3) & 1);
          if (n >= 5 && n < 33 && n % 5 == 0) issue_w2(2 + (n - 5) / 15, ((n - 5) / 5) % 3);
          if (n == 1) peek(T_F3, fl, 0);
        }
      });
    });
    SB();
  };
  round(std::integral_constant<int, 1>{});
  ST(22);
  round(std::integral_constant<int, 2>{});
  ST(23);
  round(std::integral_constant<int, 3>{});
  ST(6);
  site<3381>();
  waitfor<23, T_HS + 2, T_G + 13, T_G + 14>();
  bar();
  ST(24);
  post(1);
  if (b == 0 && wid == 0 && hvp == 0) gs8c(0, ws + E_OFF, hvp, u32x2{sfresh(T0), sfresh(T1)});
  if (!odd) {
    PATH("BEG 1");
    round(std::integral_constant<int, 4>{});
    ST(25);
    asm volatile("s_nop 3" : "+v"(cs));
    put_colsum(cs, slab(4), ng, false);
    waitfor<24, T_W + 1>();
    bar_lds();
#pragma unroll
    for (int e = 0; e < 4; ++e) asm volatile("" : "=v"(acc[1][e]));
    PATH("END 1");
  }
  asm volatile("" : "+s"(idle));
  if (idle) {
    site<3900>();
    PATH("BEG 2");
    LANES;
    const int wd = sfresh(wid), bb_ = sfresh(b);
    const u32 hvp = lane * 16, cc = lane & 15;
    peek(T_F3, fl, 0);
    u32x4 tm[6];
#pragma unroll
    for (int e = 0; e < 6; ++e) tm[e] = ldsr<u32x4>(lds + L_TM + e * 1024 + hvp);
    u32 za[3];
    SFOR(t, 3, za[t] = lds + L_Z + (64 * g + cc) * 128 + ((wd ^ (cc & 7)) * 16) + ((t + (lane >> 3)) & 3) * 4;);
    const u32 wo = (3 * wd + ((bb_ >> 5) & 1)) * 2, wd0 = (wo & 60) + ((lane >> 3) & 3) * 4, wb = lds + L_W + g * 1024 + cc * 64, wx = (cc & 6) * 8, wa0 = wb + (wd0 ^ wx), wa1 = wb + (((wd0 + 4) & 60) ^ wx);
    const u32 ws0 = 0x01000c0cu + (wo & 2) * 0x01010000u;
    u32x4 a[3];
    u32x2 wq[8];
    SFOR(u, 12, a[u % 3][u / 3] = ldsr<u32>(za[u % 3] + (u / 3) * 2048););
    SFOR(e, 4, acc[0][e] = sitev<3240 + e>(acc[1][e]););
    ST(27);
    {
      const f32x4 t0 = __builtin_bit_cast(f32x4, tm[4]), t1 = __builtin_bit_cast(f32x4, tm[5]);
      const float tc[4] = {t0[0], t0[2], t1[0], t1[2]}, ts[4] = {t0[1], t0[3], t1[1], t1[3]};
      const f32x4 z = {0.f, 0.f, 0.f, 0.f};
      f32x4 re[3], im[3];
#define STAGE(K, FN, X, Y, XI, YI) _Pragma("unroll") for (int t = 0; t < 3; ++t) { re[t] = FN(X, Y, z); im[t] = FN(XI, YI, z); } \
      SB(); if constexpr (K == 0) { SFOR(e, 6, issue_w2(2 + e / 3, e % 3);); } else if constexpr (K >= 2) { if constexpr (K == 2) { waitfor<26, T_F3>(); pin(fl); POLL(bad_flags(fl), peek(0, fl, 0), pin(fl)); } \
        SFOR(e, 6, issue_hp(std::integral_constant<int, (K >= 2 ? 6 * K - 12 + e : 0)>{});); } site<3250 + K>(); ST(55 + K + (K > 0 ? -25 : -2)); \
      asm volatile("s_nop 7" : "+v"(re[0]), "+v"(im[0]), "+v"(re[1]), "+v"(im[1]), "+v"(re[2]), "+v"(im[2]));
      STAGE(0, mfma16h, a[t], tm[0], a[t], tm[1])
      SFOR(u, 8, wq[u][0] = ldsr<u32>(wa0 + (u & 3) * 8192 + (u >> 2) * 4096); wq[u][1] = ldsr<u32>(wa1 + (u & 3) * 8192 + (u >> 2) * 4096); site<3100 + u>(););
      SFOR(t, 3, SFOR(e, 4, a[t][e] = pkhf(cmulc(re[t][e], im[t][e], tc[e], ts[e]));); a[t] = sitev<3120 + t>(a[t]););
      STAGE(1, mfma16h, tm[0], a[t], tm[1], a[t])
      SFOR(t, 3, SFOR(e, 4, a[t][e] = pkbf(cmul(re[t][e], im[t][e], __builtin_bit_cast(float, __builtin_amdgcn_perm(wq[e][1], wq[e][0], ws0 + t * 0x02020000u)), __builtin_bit_cast(float, __builtin_amdgcn_perm(wq[4 + e][1], wq[4 + e][0], ws0 + t * 0x02020000u))));); a[t] = sitev<3140 + t>(a[t]););
      STAGE(2, mfma16b, a[t], tm[2], a[t], tm[3])
      SFOR(t, 3, SFOR(e, 4, a[t][e] = pkbf(cmul(re[t][e], im[t][e], tc[e], ts[e]));); a[t] = sitev<3160 + t>(a[t]););
      STAGE(3, mfma16b, tm[2], a[t], tm[3], a[t])
#undef STAGE
      SFOR(u, 12, ldsw<u32>(za[u % 3] + (u / 3) * 2048, pkbf(re[u % 3][u / 3], im[u % 3][u / 3])); if constexpr (u % 3 == 0) site<3180 + u>(););
    }
    const u32 vs = lt(2);
    bar_lds();
    ST(28);
    {
      const u32x4 rv = srd(sfresh(ws) + V_OFF + (size_t)((bb_ >> 5) * 13 + ((bb_ & 31) >> 1)) * V_G, 32768 & tsk);
      u32x4 vo[4];
      SFOR(e, 4, vo[e] = ldsr<u32x4>(lds + L_Z + e * 8192 + wd * 1024 + hvp););
      SFOR(e, 4, bs16c(0, rv, vs, e * 8192 + wd * 1024, 0, vo[e]); site<3190 + e>(););
    }
    waitfor<25, T_W + 1>();
    bar();
    PATH("END 2");
  }
#undef RD1
#undef FRAG
  ST(7);
  site<3380>();

  LANES;
  u32 tr2[3];
  tr2[0] = lds + ng * 9216 + (lane >> 2) * 288 + p * 8; tr2[1] = tr2[0] + 3 * SLOT2; tr2[2] = tr2[0] + 6 * SLOT2; pin(tr2[0]); pin(tr2[1]); pin(tr2[2]);
  f32x4 ac[2][9];
  constexpr int PF = 5, NB = 6;
  u32x2 w2f[NB][2];
#define RD2(f, hh) w2f[(f) % NB][hh] = trread(tr2[(((f) / 9 + 4) & 7) / 3] + ((((f) / 9 + 4) & 7) % 3) * SLOT2 + ((f) % 9) * 32 + (hh) * 16 * 288)
#pragma unroll
  for (int f = 0; f < PF; ++f) { RD2(f, 0); RD2(f, 1); }
  u32x2 va[3];
  f32x4 yr[3], yi[3];
  float k0s;
  u32 yw[2], xw;
  u32x2 xo[2], bo[2], ca[2], cb[2];
  u32 xe, be, pk[8];
  float y0, y1;
  const int lsl = slab(idle ? 3 : 4);
  const u32 fso = (hs0 + lsl) * 16384;
  u32 voffv, vra, voffx, voffe, fa, fn, ea, en;
  u32x2 br, bi;
  auto tail_setup = [&](auto kc) LAM {
    constexpr int k = decltype(kc)::value;
    LANES;
    if constexpr (k == 0) voffv = lt(3);
    if constexpr (k == 2) { vra = lds + L_V + 2 * g * 1664 + (wid < 6 ? wid + 6 * (lane & 15) : 96u) * 16; k0s = tid < 16 ? 0.5f : 1.f; pin(vra); pin(k0s); }
    if constexpr (k == 3) { fa = lane & 7; fn = 144 * (wid >> 1) + 64 * (wid & 1) + 32 * (lane >> 5) + 4 * ((lane >> 3) & 3); voffx = fa * 256 * 1152 + fn * 2; pin(fa); pin(fn); pin(voffx); }
    if constexpr (k == 4) { ea = (lane >> 2) & 7; en = 144 * (wid >> 1) + 128 + 8 * (wid & 1) + 4 * (lane >> 5) + p; voffe = ea * 256 * 1152 + en * 2; pin(ea); pin(en); pin(voffe); }
    if constexpr (k == 5 || k == 6) {
      const float an = (float)(((256 * (lane & 7) + b) * (2 * g + k - 5)) & 2047) * (1.f / 2048.f), c = __builtin_amdgcn_cosf(an), s = __builtin_amdgcn_sinf(an);
      br[k - 5] = pkbf(c, -s); bi[k - 5] = pkbf(s, c); pin(br); pin(bi);
    }
    if constexpr (k == 7) {
      xw = lds + L_XC + wid * 9216 + hvp; yw[0] = wid < 7 ? lds + L_Y + (lane & 7) * YP + 3 * wid * 64 + g * 16 : lds + L_DUMP; yw[1] = wid < 6 ? yw[0] : lds + L_DUMP;
      pin(yw[0]); pin(yw[1]); pin(xw);
    }
  };
  sfor<0, 162>([&](auto fc) LAM {
    constexpr int f = decltype(fc)::value, n = f / 9, t = f % 9, fr = f + PF;
    if (t == 0) { pin(hf[n % 10][0]); pin(hf[n % 10][1]); if constexpr (n % 2 == 0) ST(40 + n / 2); }
    if constexpr (fr % 9 == 0 && n < 17) {
      SB();
      if constexpr (n & 1) {
        if constexpr (n == 1) waitfor<30 + n, T_W + n + 1, T_W + n + 2>();
        else waitfor<30 + n, T_W + n + 1, T_W + n + 2, T_H + n + 1>();
        bar();
        if (n == 3 && wid == 0 && hvp == 0) gs8c(0, ws + FV_OFF + b * 8, hvp, u32x2{sfresh(T0), sfresh(T1)});
      } else if constexpr (n == 4) { waitfor<30 + n, T_H + n + 1, T_FIN>(); bar(); post(2); }
      else if constexpr (n == 16) { waitfor<30 + n, T_H + n + 1, T_V>(); bar(); }
      else if constexpr (n > 3) waitfor<30 + n, T_H + n + 1>();
    }
    const u32x4 a = {w2f[f % NB][0][0], w2f[f % NB][0][1], w2f[f % NB][1][0], w2f[f % NB][1][1]};
    sfor<0, 2>([&](auto mc) LAM {
      constexpr int m = decltype(mc)::value, ne = 2 * f + m;
      SB();
      site<2000 + ne>();
      if (n == 0) mmaz(ac[m][t], a, hf[0][m]); else mma(ac[m][t], a, hf[n % 10][m]);
      SB();
      if (fr < 162) RD2(fr, m);
      SB();
      if constexpr (ne == 0) ca[0] = ldsr<u32x2>(cab + lsl * 1024);
      if constexpr (ne == 1) cb[0] = ldsr<u32x2>(cbb);
      if constexpr (ne == 2) cb[1] = ldsr<u32x2>(cbb + 256);
      if constexpr (ne == 3) ca[1] = ldsr<u32x2>(cab + lsl * 1024 + 256);
      if constexpr (ne >= 6 && ne < 10) mma16(acc[0][ne - 6], ca[(ne - 6) >> 1], cb[(ne - 6) & 1]);
      if constexpr (ne >= 12 && ne < 44) {
        constexpr int e = ne - 12, pr = e >> 2, tl = (pr >> 2) + (pr & 2), hh = pr & 1;
        if ((e & 3) == 0) asm volatile("v_mul_f32 %0, %1, abs(%1)" : "=v"(y0) : "v"(acc[0][tl][2 * hh]));
        if ((e & 3) == 1) asm volatile("v_mul_f32 %0, %1, abs(%1)" : "=v"(y1) : "v"(acc[0][tl][2 * hh + 1]));
        if ((e & 3) == 2) asm volatile("v_cvt_pk_bf16_f32 %0, %1, %2" : "=v"(pk[pr]) : "v"(y0), "v"(y1));
        if ((e & 3) == 3) asm volatile("v_pk_max_i16 %0, %0, 0" : "+v"(pk[pr]));
      }
      if constexpr (ne == 28 || ne == 44) { constexpr int mm = (ne - 28) / 16; bs16c(T_FIN, rh, hvp, fso, mm * 1024, u32x4{pk[4 * mm], pk[4 * mm + 1], pk[4 * mm + 2], pk[4 * mm + 3]}); }
      if constexpr (ne == 46) { if (idle) { hf[3][0] = u32x4{pk[0], pk[1], pk[2], pk[3]}; hf[3][1] = u32x4{pk[4], pk[5], pk[6], pk[7]}; } }
      constexpr int WQ[18][2] = {{4, -1}, {5, -1}, {6, 7}, {8, -1}, {9, -1}, {10, -1}, {11, -1}, {12, -1}, {13, -1}, {14, -1}, {15, -1}, {16, -1}, {17, -1}, {-1, -1}, {-1, -1}, {-1, -1}, {-1, -1}, {-1, -1}};
      if constexpr (m == 1 && (t == 0 || t == 2 || t == 4) && WQ[n][0] >= 0) issue_w2(WQ[n][0], t >> 1);
      if constexpr (m == 1 && (t == 1 || t == 3 || t == 5) && WQ[n][1] >= 0) issue_w2(WQ[n][1], t >> 1);
      if constexpr (m == 1 && (t == 6 || t == 8) && n >= 6 && n != 9 && n + 4 < 18) issue_h(n + 4, (t - 6) >> 1);
      if constexpr (m == 1 && n == 10 && (t == 1 || t == 3)) issue_h(13, (t - 1) >> 1);
      if constexpr (m == 0) {
        if constexpr (n >= 4 && n < 12 && t == 7) tail_setup(std::integral_constant<int, n - 4>{});
        if constexpr (n == 6 && t == 1) { waitfor<50, T_FA>(); pin(fl); POLL(bad_flags(fl), peek(0, fl, 1), pin(fl)); }
        if constexpr (n == 10 && t == 1) { waitfor<51, T_FB>(); pin(fl); POLL(bad_flags(fl), peek(0, fl, 2), pin(fl)); }
        if constexpr (n == 13 && t == 3) { waitfor<52, T_FALL>(); pin(fl); POLL(bad_flags(fl), gl16c(0, fl, ws + FV_OFF + wid * 256, vand<255>(hvp)), pin(fl)); }
        if (n == 3 && t == 0) peek(T_FA, fl, 1);
        if (n == 7 && t == 1) peek(T_FB, fl, 2);
        if (n == 10 && t == 2) gl16c(T_FALL, fl, ws + FV_OFF + wid * 256, vand<255>(hvp));
        if constexpr (n == 14 && t < 2) bldsc(T_V, srd(ws + V_OFF, 104 * (u32)V_G), voffv, wid * 8 * (u32)V_G + b * 128, t * 64 * (u32)V_G, dstw, L_V + t * 8192);
        if constexpr (n == 16 && t >= 5 && t < 8) { const u32 o = vra + ((t - 5 + zrot) & 3) * 4; va[t - 5][0] = ldsr<u32>(o); va[t - 5][1] = ldsr<u32>(o + 1664); }
        if constexpr (n == 17 && t < 3) { mmaz16(yr[t], va[t], br); mmaz16(yi[t], va[t], bi); }
        if constexpr (n == 17 && t == 4) { pin(yr[0]); yr[0][0] *= k0s; }
        if constexpr (n == 17 && t >= 5 && t < 8) { constexpr int u = t - 5; pin(yr[u]); pin(yi[u]); ldsw<u32x4>(yw[u > 0] + u * 64, u32x4{pkbf(yr[u][0], yi[u][0]), pkbf(yr[u][1], yi[u][1]), pkbf(yr[u][2], yi[u][2]), pkbf(yr[u][3], yi[u][3])}); }
        if constexpr (n == 17 && t >= 3) { pin(ac[0][t - 3]); ldsw<f32x4>(xw + (t - 3) * 1024, ac[0][t - 3]); }
        if constexpr (n == 14 && (t == 3 || t == 5)) { const u32 o = umin(2 * sfresh(wid) + t / 4, 11) * 1024; glds(T_TI, tb + TB_INV * 1024 + o, hvp, lds + o, L_TI); }
        if constexpr (n == 15 && t < 2) gl8(T_IN, xo[t], xp + t * 32, voffx);
        if constexpr (n == 15 && t == 2) gl2(T_IN, xe, xp, voffe);
        if constexpr (n == 15 && (t == 3 || t == 5)) gl8(T_IN, bo[(t - 3) / 2], b2p + (t - 3) * 16, fn * 2);
        if constexpr (n == 15 && t == 7) gl2(T_IN, be, b2p, en * 2);
      }
    });
  });
  SB();
#undef RD2
  ST(8);
  site<3379>();
  u32x4 ti_[12];
  {
    const u32 xr = lds + L_XC + (wid ^ 4) * 9216 + hvp;
    waitfor<53, T_TI>();
    asm volatile("s_nop 5" ::: "memory");
    SFOR(t, 3, pin(ac[0][6 + t]); ldsw<f32x4>(xw + (6 + t) * 1024, ac[0][6 + t]); site<3300 + t>(););
    bar_lds();
    char* pd = ws + P_OFF + (size_t)((i_ * 16 + 4 * mg + 2 * (1 - ng)) * 4 + q_) * P_BLK + r_ * 2304;
    const u32 po = ((lane >> 3) & 1) * 4 * P_BLK, pi = g * 8 + (lane & 7), pv = po + pi * 16;
    f32x4 s[9];
    SFOR(t, 9, s[t] = ldsr<f32x4>(xr + t * 1024); SB(); site<3310 + t>(););
    SFOR(tp, 4,
      const f32x4 a = sitev<3320 + tp>(ac[1][2 * tp] + s[2 * tp]); const f32x4 c = sitev<3325 + tp>(ac[1][2 * tp + 1] + s[2 * tp + 1]);
      gs16c(0, pd + tp * 512, pv, u32x4{pkbf(a[0], a[1]), pkbf(a[2], a[3]), pkbf(c[0], c[1]), pkbf(c[2], c[3])}); site<3330 + tp>();
    );
    const f32x4 a = ac[1][8] + s[8];
    gs8c(0, pd + 2048, po + pi * 8, u32x2{pkbf(a[0], a[1]), pkbf(a[2], a[3])});
    SFOR(e, 12, ti_[e] = ldsr<u32x4>(lds + L_TI + e * 1024 + hvp); if constexpr (e % 3 == 0) { SB(); site<3340 + e>(); });
  }
  ST(9);
  site<3375>();
  const char* ib = ws + P_OFF + (size_t)(i_ * 16 + j_) * 4 * P_BLK + (wid >> 1) * 2304;
  u32x4 pa[4];
  u32 pe[4];
  auto load_in = [&]() LAM {
#pragma unroll
    for (int s = 0; s < 4; ++s) { gl16c(0, pa[s], ib + s * P_BLK + (wid & 1) * 1024, hvp); gl2c(0, pe[s], ib + s * P_BLK + 2048 + (wid & 1) * 128, lane * 2); }
  };
  {
    const u32 h = lane >> 5, rr = lane & 31;
    const u32 ya = lds + L_Y + wid * YP + umin(rr, 17u) * 64 + h * 16;
    const u32x4 A0 = ldsr<u32x4>(ya), A1 = ldsr<u32x4>(ya + 32);
    f32x16 z; for (int e = 0; e < 16; ++e) z[e] = 0.f;
    const float ny = bflo(ldsr<u32>(lds + L_Y + wid * YP + 18 * 64));
    f32x16 vr = mfma32b(A0, ti_[0], z), vi = mfma32b(A0, ti_[2], z);
    vr = mfma32b(A1, ti_[1], vr); vi = mfma32b(A1, ti_[3], vi);
    settle(vr, vi);
    vr[0] += ny * (h ? 0.f : ((lane & 1) ? -0.5f : 0.5f));
    u32 pq[12];
    SFOR(e, 5,
      const f32x4 tw = __builtin_bit_cast(f32x4, ti_[4 + e]);
      pq[2 * e] = sitev<3360 + 2 * e>(pkbf(cmul(vr[2 * e], vi[2 * e], tw[0], tw[1])));
      pq[2 * e + 1] = sitev<3361 + 2 * e>(pkbf(cmul(vr[2 * e + 1], vi[2 * e + 1], tw[2], tw[3])));
    );
    pq[10] = 0; pq[11] = 0;
    f32x16 y = z;
#pragma unroll
    for (int s = 0; s < 3; ++s) y = mfma32b(ti_[9 + s], u32x4{pq[4 * s], pq[4 * s + 1], pq[4 * s + 2], pq[4 * s + 3]}, y);
    SB();
    WAIT_V(0);
    pin(xo[0]); pin(xo[1]); pin(bo[0]); pin(bo[1]); pin(xe); pin(be);
    settle(y);
#pragma unroll
    for (int e = 0; e < 10; ++e) {
      const u32 d1 = (e & 3) + 8 * (e >> 2) + 4 * h;
      if (e < 8 || h == 0) ldsw<float>(lds + L_S + (wid * 576 + 32 * d1 + rr) * 4, y[e]);
    }
  }
  bar_lds();
  ST(10);
  site<3376>();
  post(3);
  const f32x4 sv0 = ldsr<f32x4>(lds + L_S + (fa * 576 + fn) * 4), sv1 = ldsr<f32x4>(lds + L_S + (fa * 576 + fn + 16) * 4);
  const float se = ldsr<float>(lds + L_S + (ea * 576 + en) * 4), i2 = ldsr<float>(lds + L_RI + fa * 4), i2e = ldsr<float>(lds + L_RI + ea * 4);
#define PINS pin(pa[0]); pin(pa[1]); pin(pa[2]); pin(pa[3]); pin(pe[0]); pin(pe[1]); pin(pe[2]); pin(pe[3])
  if (wid == 0) {
    int sp = 0;
    do { peek(0, fl, 3); WAIT_V(0); pin(fl); } while (anylane(bad_flags(fl)) && ++sp < SPIN_CAP);

  }
  bar();
  ST(13);
  site<3377>();
  load_in();
  WAIT_V(0);
  PINS;
#undef PINS
  ST(11);
  site<3378>();
  const u32 sell = sfresh(0x00003f80u), selh = sfresh(0x3f800000u);
  auto gate = [&](float sum, float sc, float bias, float s, float xv) LAM {
    const float o = __builtin_fmaf(sum, sc, bias);
    return __builtin_fmaf(s, o * __builtin_amdgcn_rcpf(1.f + __builtin_amdgcn_exp2f(-1.4426950408889634f * o)), xv);
  };
  SFOR(t, 2,
    float res[4];
    SFOR(e, 4,
      float sum = 0.f;
      for (int s = 0; s < 4; ++s) sum = dot2(pa[s][2 * t + (e >> 1)], (e & 1) ? selh : sell, sum);
      res[e] = sitev<3400 + 8 * t + e>(gate(sitev<3404 + 8 * t + e>(sum), i2, (e & 1) ? bfhi(bo[t][e >> 1]) : bflo(bo[t][e >> 1]), t ? sv1[e] : sv0[e], (e & 1) ? bfhi(xo[t][e >> 1]) : bflo(xo[t][e >> 1])));
    );
    gs8(0, outp + t * 32, voffx, u32x2{pkbf(res[0], res[1]), pkbf(res[2], res[3])});
  );
  {
    const float r = gate((bflo(pe[0]) + bflo(pe[1])) + (bflo(pe[2]) + bflo(pe[3])), i2e, bflo(be), se, bflo(xe));
    gs2(0, outp, voffe, pkbf(r, r));
  }
  site<3420>();
  ST(12);
}

extern "C" void kernel_launch(void* const* d_in, const int* in_sizes, int n_in, void* d_out, int out_size, void* d_ws, size_t ws_size,
                              hipStream_t stream) {
  (void)hipFuncSetAttribute((const void*)k_all, hipFuncAttributeMaxDynamicSharedMemorySize, L_END);
  k_all<<<256, 512, L_END, stream>>>((const u16*)d_in[0], (const u16*)d_in[1], (const u16*)d_in[2], (const u16*)d_in[4], (const u16*)d_in[5],
                                     (const u16*)d_in[6], (const u16*)d_in[7], (char*)d_ws, (u16*)d_out);
}
```
